```python
import torch

def _softmax(x, axis=-1):
    return torch.softmax(x, dim=axis)
def _gelu(x):
    return torch.nn.functional.gelu(x, approximate="tanh")
def _silu(x):
    return torch.nn.functional.silu(x)
def _sigmoid(x):
    return torch.sigmoid(x)
def _layernorm(x, axis=-1, eps=1e-5):
    m = x.mean(dim=axis, keepdim=True)
    v = x.var(dim=axis, keepdim=True, unbiased=False)
    return (x - m) / torch.sqrt(v + eps)
def _scatter_add(tgt, idx, src, axis=0):
    return tgt.clone().index_add_(axis, idx, src)
def _segment_sum(src, seg, n):
    out = torch.zeros((n,) + src.shape[1:], dtype=src.dtype)
    return out.index_add_(0, seg, src)
def _pad(x, pw):
    flat = tuple(v for pair in reversed(pw) for v in pair)
    return torch.nn.functional.pad(x, flat)

kernel_name = 'standalone__gqa_block_cached__near_ridge'

def setup_inputs():

    _w = (lambda a: (lambda t: t.to(torch.bfloat16) if t.is_floating_point() else t)(torch.as_tensor(a)))
    B, Sq, D = 16, 64, 512
    Hq, Hkv, Sk, Dh = 32, 8, 1024, 128
    assert Hq % Hkv == 0, (Hq, Hkv)
    return {
        "x": _w((lambda *s: torch.randn(*s, dtype=torch.bfloat16) * 0.8)(B, Sq, D)),
        "k_cache": _w((lambda *s: torch.randn(*s, dtype=torch.bfloat16) * 0.8)(B, Hkv, Sk, Dh)),
        "v_cache": _w((lambda *s: torch.randn(*s, dtype=torch.bfloat16) * 0.05)(B, Hkv, Sk, Dh)),
        "wq": _w((lambda *s: torch.randn(*s, dtype=torch.bfloat16) * 0.8)(D, Hq * Dh) * (D ** -0.5)),
        "wo": _w((lambda *s: torch.randn(*s, dtype=torch.bfloat16) * 0.05)(Hq * Dh, D) * ((Hq * Dh) ** -0.5)),
    }

def reference(x, k_cache, v_cache, wq, wo):
    B, Sq, D = x.shape
    Hq, Dh = 32, 128
    Hkv = k_cache.shape[1]
    rep = Hq // Hkv
    q = (lambda x, *d: x.permute(*d))((x @ wq).reshape(B, Sq, Hq, Dh), 0, 2, 1, 3)
    k = (lambda x, n, axis: torch.repeat_interleave(x, n, dim=axis))(k_cache, rep, axis=1)
    v = (lambda x, n, axis: torch.repeat_interleave(x, n, dim=axis))(v_cache, rep, axis=1)
    scores = torch.einsum("bhqd,bhkd->bhqk", q, k) * (Dh ** -0.5)
    p = _softmax(scores, axis=-1)
    o = torch.einsum("bhqk,bhkd->bhqd", p, v)
    return (lambda x, *d: x.permute(*d))(o, 0, 2, 1, 3).reshape(B, Sq, Hq * Dh) @ wo
```

```cpp
#include <hip/hip_runtime.h>
#include <hip/hip_bf16.h>
typedef __hip_bfloat16 bf16;
constexpr int B=16,SQ=64,D=512,HQ=32,HKV=8,SK=1024,DH=128;
__device__ __forceinline__ float b2f(bf16 v){return __bfloat162float(v);}
__global__ void qproj(const bf16* x,const bf16* wq,bf16* q){
  int n=blockIdx.x*256+threadIdx.x; int m=blockIdx.y;
  float acc=0; for(int k=0;k<D;++k) acc+=b2f(x[m*D+k])*b2f(wq[(size_t)k*4096+n]);
  q[(size_t)m*4096+n]=__float2bfloat16(acc);
}
__global__ void attn(const bf16* q,const bf16* K,const bf16* V,bf16* o){
  __shared__ float s[SK]; __shared__ float qs[DH]; __shared__ float red[256];
  int row=blockIdx.x;
  int h=blockIdx.y; int b=row/SQ; int kvh=h/4; int t=threadIdx.x;
  if(t<DH) qs[t]=b2f(q[(size_t)row*4096+h*DH+t]);
  __syncthreads();
  const bf16* Kb=K+((size_t)(b*HKV+kvh))*SK*DH; const bf16* Vb=V+((size_t)(b*HKV+kvh))*SK*DH;
  float mx=-1e30f;
  for(int k=t;k<SK;k+=256){float a=0; for(int d=0;d<DH;++d)a+=qs[d]*b2f(Kb[(size_t)k*DH+d]); a*=0.08838834764831845f; s[k]=a; mx=fmaxf(mx,a);}
  red[t]=mx; __syncthreads();
  for(int o2=128;o2>0;o2>>=1){ if(t<o2) red[t]=fmaxf(red[t],red[t+o2]); __syncthreads(); }
  mx=red[0]; __syncthreads();
  float sum=0; for(int k=t;k<SK;k+=256){float e=__expf(s[k]-mx); s[k]=e; sum+=e;}
  red[t]=sum; __syncthreads();
  for(int o2=128;o2>0;o2>>=1){ if(t<o2) red[t]+=red[t+o2]; __syncthreads(); }
  sum=red[0];
  if(t<DH){ float a=0; for(int k=0;k<SK;++k) a+=s[k]*b2f(Vb[(size_t)k*DH+t]); o[(size_t)row*4096+h*DH+t]=__float2bfloat16(a/sum); }
}
__global__ void oproj(const bf16* o,const bf16* wo,bf16* out){
  int n=blockIdx.x*256+threadIdx.x; int m=blockIdx.y;
  float acc=0; for(int k=0;k<4096;++k) acc+=b2f(o[(size_t)m*4096+k])*b2f(wo[(size_t)k*D+n]);
  out[(size_t)m*D+n]=__float2bfloat16(acc);
}
extern "C" void kernel_launch(void* const* d_in, const int* in_sizes, int n_in, void* d_out, int out_size, void* d_ws, size_t ws_size, hipStream_t stream) {
  const bf16* x=(const bf16*)d_in[0]; const bf16* K=(const bf16*)d_in[1]; const bf16* V=(const bf16*)d_in[2]; const bf16* wq=(const bf16*)d_in[3]; const bf16* wo=(const bf16*)d_in[4];
  bf16* q=(bf16*)d_ws; bf16* o=q+1024*4096;
  qproj<<<dim3(16,1024),256,0,stream>>>(x,wq,q);
  attn<<<dim3(1024,32),256,0,stream>>>(q,K,V,o);
  oproj<<<dim3(2,1024),256,0,stream>>>(o,wo,(bf16*)d_out);
}
```

```cpp
#include <hip/hip_runtime.h>
#include <hip/hip_bf16.h>

extern "C" __global__ __launch_bounds__(256, 1)
void gqa_main(const char* __restrict__ x, const char* __restrict__ kc, const char* __restrict__ vc, const char* __restrict__ wq,
              const char* __restrict__ wo, char* __restrict__ out, char* __restrict__ ws) {
  extern __shared__ __attribute__((aligned(16))) char lds[];
  unsigned tid = threadIdx.x, bid = blockIdx.x;
  asm volatile(
    "s_mov_b32 s20, %7\n\t"
    "v_lshrrev_b32 v2, 6, %8\n\t"
    "v_and_b32 v1, 63, %8\n\t"
    "v_readfirstlane_b32 s21, v2\n\t"
    "LCS_%=:\n\t"
    "s_getpc_b64 s[72:73]\n\t"
    "v_lshlrev_b32 v2, 7, v1\n\t"
    "s_lshl_b32 s26, s21, 13\n\t"
    "v_add_u32 v2, s26, v2\n\t"
    "s_mov_b32 s27, LEND_%=-LCS_%=\n\t"
    "s_mov_b64 s[98:99], exec\n\t"
    "v_cmp_gt_u32 vcc, s27, v2\n\t"
    "s_and_b64 exec, vcc, exec\n\t"
    "s_cbranch_execz LPFX0_%=\n\t"
    "global_load_dword v81, v2, s[72:73]\n\t"
    "LPFX0_%=:\n\t"
    "s_mov_b64 exec, s[98:99]\n\t"
    "v_add_u32 v2, 32768, v2\n\t"
    "v_cmp_gt_u32 vcc, s27, v2\n\t"
    "s_and_b64 exec, vcc, exec\n\t"
    "s_cbranch_execz LPFX1_%=\n\t"
    "global_load_dword v81, v2, s[72:73]\n\t"
    "LPFX1_%=:\n\t"
    "s_mov_b64 exec, s[98:99]\n\t"
    "v_add_u32 v2, 32768, v2\n\t"
    "s_lshr_b32 s25, s20, 3\n\t"
    "s_lshr_b32 s23, s20, 4\n\t"
    "s_lshl_b32 s26, s23, 16\n\t"
    "s_mov_b64 s[72:73], %0\n\t"
    "s_add_u32 s72, s72, s26\n\t"
    "s_addc_u32 s73, s73, 0\n\t"
    "s_mov_b32 s36, s72\n\t"
    "s_and_b32 s37, s73, 0xffff\n\t"
    "s_mov_b32 s38, 0x10000\n\t"
    "s_mov_b32 s39, 0x20000\n\t"
    "s_and_b32 s84, s23, 3\n\t"
    "v_and_b32 v4, 15, v1\n\t"
    "v_lshrrev_b32 v5, 4, v1\n\t"
    "v_add_u32 v9, 0, v5\n\t"
    "v_xor_b32 v9, v9, v4\n\t"
    "v_lshlrev_b32 v9, 4, v9\n\t"
    "v_lshl_add_u32 v9, v5, 10, v9\n\t"
    "v_add_u32 v12, 0, v9\n\t"
    "v_add_u32 v9, 4, v5\n\t"
    "v_xor_b32 v9, v9, v4\n\t"
    "v_lshlrev_b32 v9, 4, v9\n\t"
    "v_lshl_add_u32 v9, v5, 10, v9\n\t"
    "v_add_u32 v13, 3072, v9\n\t"
    "v_add_u32 v9, 8, v5\n\t"
    "v_xor_b32 v9, v9, v4\n\t"
    "v_lshlrev_b32 v9, 4, v9\n\t"
    "v_lshl_add_u32 v9, v5, 10, v9\n\t"
    "v_add_u32 v14, 6144, v9\n\t"
    "v_add_u32 v9, 12, v5\n\t"
    "v_xor_b32 v9, v9, v4\n\t"
    "v_lshlrev_b32 v9, 4, v9\n\t"
    "v_lshl_add_u32 v9, v5, 10, v9\n\t"
    "v_add_u32 v15, 9216, v9\n\t"
    "s_lshl_b32 s75, s21, 12\n\t"
    "s_lshl_b32 s35, s21, 14\n\t"
    "s_add_u32 m0, s75, 98304\n\t"
    "s_add_u32 s34, s84, 0\n\t"
    "s_and_b32 s34, s34, 3\n\t"
    "s_lshl_b32 s34, s34, 8\n\t"
    "s_add_u32 s34, s34, s35\n\t"
    "buffer_load_dwordx4 v12, s[36:39], s34 offen lds\n\t"
    "buffer_load_dwordx4 v13, s[36:39], s34 offen offset:1024 lds\n\t"
    "buffer_load_dwordx4 v14, s[36:39], s34 offen offset:2048 lds\n\t"
    "buffer_load_dwordx4 v15, s[36:39], s34 offen offset:3072 lds\n\t"
    "s_and_b32 s22, s20, 7\n\t"
    "s_and_b32 s24, s25, 1\n\t"
    "s_lshr_b32 s82, s21, 1\n\t"
    "s_and_b32 s83, s21, 1\n\t"
    "s_lshl_b32 s26, s22, 2\n\t"
    "s_lshl_b32 s27, s24, 1\n\t"
    "s_add_u32 s26, s26, s27\n\t"
    "s_add_u32 s26, s26, s82\n\t"
    "s_lshl_b32 s26, s26, 8\n\t"
    "s_sub_u32 s27, 0x400000, s26\n\t"
    "s_mov_b64 s[72:73], %3\n\t"
    "s_add_u32 s72, s72, s26\n\t"
    "s_addc_u32 s73, s73, 0\n\t"
    "s_mov_b32 s40, s72\n\t"
    "s_and_b32 s41, s73, 0xffff\n\t"
    "s_mov_b32 s42, s27\n\t"
    "s_mov_b32 s43, 0x20000\n\t"
    "v_lshlrev_b32 v9, 2, v5\n\t"
    "v_xor_b32 v9, v9, v4\n\t"
    "v_lshlrev_b32 v9, 4, v9\n\t"
    "v_lshl_add_u32 v9, v5, 13, v9\n\t"
    "s_lshl_b32 s26, s83, 16\n\t"
    "v_add_u32 v111, s26, v9\n\t"
    "v_add_u32 v112, 31744, v111\n\t"
    "s_lshl_b32 s28, s82, 13\n\t"
    "s_lshl_b32 s26, s83, 11\n\t"
    "s_add_u32 s28, s28, s26\n\t"
    "s_add_u32 s28, s28, 32768\n\t"
    "s_lshl_b32 s74, s84, 20\n\t"
    "s_add_u32 m0, s28, 0\n\t"
    "s_add_u32 s85, s74, 0\n\t"
    "buffer_load_dwordx4 v111, s[40:43], s85 offen lds\n\t"
    "buffer_load_dwordx4 v112, s[40:43], s85 offen offset:1024 lds\n\t"
    "s_add_u32 m0, s28, 4096\n\t"
    "s_add_u32 s85, s74, 131072\n\t"
    "buffer_load_dwordx4 v111, s[40:43], s85 offen lds\n\t"
    "buffer_load_dwordx4 v112, s[40:43], s85 offen offset:1024 lds\n\t"
    "s_add_u32 s74, s74, 262144\n\t"
    "s_and_b32 s74, s74, 4194303\n\t"
    "s_add_u32 m0, s28, 16384\n\t"
    "s_add_u32 s85, s74, 0\n\t"
    "buffer_load_dwordx4 v111, s[40:43], s85 offen lds\n\t"
    "buffer_load_dwordx4 v112, s[40:43], s85 offen offset:1024 lds\n\t"
    "s_add_u32 m0, s28, 20480\n\t"
    "s_add_u32 s85, s74, 131072\n\t"
    "buffer_load_dwordx4 v111, s[40:43], s85 offen lds\n\t"
    "buffer_load_dwordx4 v112, s[40:43], s85 offen offset:1024 lds\n\t"
    "s_add_u32 s74, s74, 262144\n\t"
    "s_and_b32 s74, s74, 4194303\n\t"
    "s_lshl_b32 s80, s21, 13\n\t"
    "s_lshl_b32 s26, s23, 3\n\t"
    "s_add_u32 s26, s26, s22\n\t"
    "s_lshl_b32 s26, s26, 18\n\t"
    "s_mov_b64 s[72:73], %1\n\t"
    "s_add_u32 s72, s72, s26\n\t"
    "s_addc_u32 s73, s73, 0\n\t"
    "s_mov_b32 s44, s72\n\t"
    "s_and_b32 s45, s73, 0xffff\n\t"
    "s_mov_b32 s46, 0x40000\n\t"
    "s_mov_b32 s47, 0x20000\n\t"
    "s_mov_b64 s[72:73], %2\n\t"
    "s_add_u32 s72, s72, s26\n\t"
    "s_addc_u32 s73, s73, 0\n\t"
    "s_mov_b32 s48, s72\n\t"
    "s_and_b32 s49, s73, 0xffff\n\t"
    "s_mov_b32 s50, 0x40000\n\t"
    "s_mov_b32 s51, 0x20000\n\t"
    "s_add_u32 m0, s28, 32768\n\t"
    "s_add_u32 s85, s74, 0\n\t"
    "buffer_load_dwordx4 v111, s[40:43], s85 offen lds\n\t"
    "buffer_load_dwordx4 v112, s[40:43], s85 offen offset:1024 lds\n\t"
    "s_add_u32 m0, s28, 36864\n\t"
    "s_add_u32 s85, s74, 131072\n\t"
    "buffer_load_dwordx4 v111, s[40:43], s85 offen lds\n\t"
    "buffer_load_dwordx4 v112, s[40:43], s85 offen offset:1024 lds\n\t"
    "s_add_u32 s74, s74, 262144\n\t"
    "s_and_b32 s74, s74, 4194303\n\t"
    "s_add_u32 m0, s28, 49152\n\t"
    "s_add_u32 s85, s74, 0\n\t"
    "buffer_load_dwordx4 v111, s[40:43], s85 offen lds\n\t"
    "buffer_load_dwordx4 v112, s[40:43], s85 offen offset:1024 lds\n\t"
    "s_add_u32 m0, s28, 53248\n\t"
    "s_add_u32 s85, s74, 131072\n\t"
    "buffer_load_dwordx4 v111, s[40:43], s85 offen lds\n\t"
    "buffer_load_dwordx4 v112, s[40:43], s85 offen offset:1024 lds\n\t"
    "s_add_u32 s74, s74, 262144\n\t"
    "s_and_b32 s74, s74, 4194303\n\t"
    "v_and_b32 v2, 31, v1\n\t"
    "v_lshrrev_b32 v3, 5, v1\n\t"
    "v_and_b32 v4, 15, v1\n\t"
    "v_lshrrev_b32 v5, 4, v1\n\t"
    "v_bfe_u32 v6, v1, 2, 2\n\t"
    "v_and_b32 v7, 3, v1\n\t"
    "v_bfe_u32 v8, v1, 4, 1\n\t"
    "v_lshlrev_b32 v120, 4, v1\n\t"
    "s_lshl_b32 s77, s82, 13\n\t"
    "s_add_u32 s77, s77, 32768\n\t"
    "v_lshl_add_u32 v9, v3, 3, v6\n\t"
    "v_lshlrev_b32 v9, 8, v9\n\t"
    "v_and_b32 v10, 1, v7\n\t"
    "v_lshl_add_u32 v10, v8, 1, v10\n\t"
    "v_lshl_add_u32 v9, v10, 4, v9\n\t"
    "v_lshrrev_b32 v10, 1, v7\n\t"
    "v_lshl_add_u32 v9, v10, 3, v9\n\t"
    "v_add_u32 v9, s77, v9\n\t"
    "v_xor_b32 v10, 0, v6\n\t"
    "v_lshl_add_u32 v116, v10, 6, v9\n\t"
    "v_add_u32 v124, 65536, v116\n\t"
    "v_xor_b32 v10, 1, v6\n\t"
    "v_lshl_add_u32 v117, v10, 6, v9\n\t"
    "v_add_u32 v125, 65536, v117\n\t"
    "v_xor_b32 v10, 2, v6\n\t"
    "v_lshl_add_u32 v118, v10, 6, v9\n\t"
    "v_add_u32 v126, 65536, v118\n\t"
    "v_xor_b32 v10, 3, v6\n\t"
    "v_lshl_add_u32 v119, v10, 6, v9\n\t"
    "v_add_u32 v127, 65536, v119\n\t"
    "v_add_u32 v9, 0, v3\n\t"
    "v_xor_b32 v9, v9, v4\n\t"
    "v_lshlrev_b32 v9, 4, v9\n\t"
    "v_lshl_add_u32 v82, v2, 8, v9\n\t"
    "v_add_u32 v90, 65536, v82\n\t"
    "v_add_u32 v9, 2, v3\n\t"
    "v_xor_b32 v9, v9, v4\n\t"
    "v_lshlrev_b32 v9, 4, v9\n\t"
    "v_lshl_add_u32 v83, v2, 8, v9\n\t"
    "v_add_u32 v91, 65536, v83\n\t"
    "v_add_u32 v9, 4, v3\n\t"
    "v_xor_b32 v9, v9, v4\n\t"
    "v_lshlrev_b32 v9, 4, v9\n\t"
    "v_lshl_add_u32 v84, v2, 8, v9\n\t"
    "v_add_u32 v92, 65536, v84\n\t"
    "v_add_u32 v9, 6, v3\n\t"
    "v_xor_b32 v9, v9, v4\n\t"
    "v_lshlrev_b32 v9, 4, v9\n\t"
    "v_lshl_add_u32 v85, v2, 8, v9\n\t"
    "v_add_u32 v93, 65536, v85\n\t"
    "v_add_u32 v9, 8, v3\n\t"
    "v_xor_b32 v9, v9, v4\n\t"
    "v_lshlrev_b32 v9, 4, v9\n\t"
    "v_lshl_add_u32 v86, v2, 8, v9\n\t"
    "v_add_u32 v94, 65536, v86\n\t"
    "v_add_u32 v9, 10, v3\n\t"
    "v_xor_b32 v9, v9, v4\n\t"
    "v_lshlrev_b32 v9, 4, v9\n\t"
    "v_lshl_add_u32 v87, v2, 8, v9\n\t"
    "v_add_u32 v95, 65536, v87\n\t"
    "v_add_u32 v9, 12, v3\n\t"
    "v_xor_b32 v9, v9, v4\n\t"
    "v_lshlrev_b32 v9, 4, v9\n\t"
    "v_lshl_add_u32 v88, v2, 8, v9\n\t"
    "v_add_u32 v96, 65536, v88\n\t"
    "v_add_u32 v9, 14, v3\n\t"
    "v_xor_b32 v9, v9, v4\n\t"
    "v_lshlrev_b32 v9, 4, v9\n\t"
    "v_lshl_add_u32 v89, v2, 8, v9\n\t"
    "v_add_u32 v97, 65536, v89\n\t"
    "s_add_u32 m0, s28, 81920\n\t"
    "s_add_u32 s85, s74, 0\n\t"
    "buffer_load_dwordx4 v111, s[40:43], s85 offen lds\n\t"
    "buffer_load_dwordx4 v112, s[40:43], s85 offen offset:1024 lds\n\t"
    "s_add_u32 m0, s28, 86016\n\t"
    "s_add_u32 s85, s74, 131072\n\t"
    "buffer_load_dwordx4 v111, s[40:43], s85 offen lds\n\t"
    "buffer_load_dwordx4 v112, s[40:43], s85 offen offset:1024 lds\n\t"
    "s_add_u32 s74, s74, 262144\n\t"
    "s_and_b32 s74, s74, 4194303\n\t"
    "s_add_u32 m0, s28, 98304\n\t"
    "s_add_u32 s85, s74, 0\n\t"
    "buffer_load_dwordx4 v111, s[40:43], s85 offen lds\n\t"
    "buffer_load_dwordx4 v112, s[40:43], s85 offen offset:1024 lds\n\t"
    "s_add_u32 m0, s28, 102400\n\t"
    "s_add_u32 s85, s74, 131072\n\t"
    "buffer_load_dwordx4 v111, s[40:43], s85 offen lds\n\t"
    "buffer_load_dwordx4 v112, s[40:43], s85 offen offset:1024 lds\n\t"
    "s_add_u32 s74, s74, 262144\n\t"
    "s_and_b32 s74, s74, 4194303\n\t"
    "s_waitcnt vmcnt(20)\n\t"
    "s_barrier\n\t"
    "s_lshl_b32 s26, s83, 13\n\t"
    "s_add_u32 s26, s26, 98304\n\t"
    "v_add_u32 v9, s26, v82\n\t"
    "ds_read_b128 v[128:131], v9\n\t"
    "v_add_u32 v10, s26, v83\n\t"
    "ds_read_b128 v[132:135], v10\n\t"
    "v_add_u32 v11, s26, v84\n\t"
    "ds_read_b128 v[136:139], v11\n\t"
    "v_add_u32 v9, s26, v85\n\t"
    "ds_read_b128 v[140:143], v9\n\t"
    "v_add_u32 v10, s26, v86\n\t"
    "ds_read_b128 v[144:147], v10\n\t"
    "v_add_u32 v11, s26, v87\n\t"
    "ds_read_b128 v[148:151], v11\n\t"
    "v_add_u32 v9, s26, v88\n\t"
    "ds_read_b128 v[152:155], v9\n\t"
    "v_add_u32 v10, s26, v89\n\t"
    "ds_read_b128 v[156:159], v10\n\t"
    "ds_read_b64_tr_b16 a[128:129], v116\n\t"
    "ds_read_b64_tr_b16 a[130:131], v116 offset:1024\n\t"
    "ds_read_b64_tr_b16 a[132:133], v117\n\t"
    "ds_read_b64_tr_b16 a[134:135], v117 offset:1024\n\t"
    "ds_read_b64_tr_b16 a[136:137], v118\n\t"
    "ds_read_b64_tr_b16 a[138:139], v118 offset:1024\n\t"
    "ds_read_b64_tr_b16 a[140:141], v119\n\t"
    "ds_read_b64_tr_b16 a[142:143], v119 offset:1024\n\t"
    "ds_read_b64_tr_b16 a[144:145], v116 offset:4096\n\t"
    "ds_read_b64_tr_b16 a[146:147], v116 offset:5120\n\t"
    "ds_read_b64_tr_b16 a[148:149], v117 offset:4096\n\t"
    "ds_read_b64_tr_b16 a[150:151], v117 offset:5120\n\t"
    "ds_read_b64_tr_b16 a[152:153], v118 offset:4096\n\t"
    "ds_read_b64_tr_b16 a[154:155], v118 offset:5120\n\t"
    "ds_read_b64_tr_b16 a[156:157], v119 offset:4096\n\t"
    "ds_read_b64_tr_b16 a[158:159], v119 offset:5120\n\t"
    "s_waitcnt vmcnt(16) lgkmcnt(0)\n\t"
    "s_barrier\n\t"
    "s_cmp_lt_u32 s21, 2\n\t"
    "s_cselect_b32 s64, s44, s48\n\t"
    "s_cselect_b32 s65, s45, s49\n\t"
    "s_mov_b32 s66, 0x40000\n\t"
    "s_mov_b32 s67, 0x20000\n\t"
    "s_lshl_b32 s26, s22, 19\n\t"
    "s_lshl_b32 s27, s24, 18\n\t"
    "s_add_u32 s26, s26, s27\n\t"
    "s_lshl_b32 s27, s21, 8\n\t"
    "s_add_u32 s26, s26, s27\n\t"
    "s_lshl_b32 s27, s21, 8\n\t"
    "s_sub_u32 s27, 0x40000, s27\n\t"
    "s_mov_b64 s[72:73], %4\n\t"
    "s_add_u32 s72, s72, s26\n\t"
    "s_addc_u32 s73, s73, 0\n\t"
    "s_mov_b32 s52, s72\n\t"
    "s_and_b32 s53, s73, 0xffff\n\t"
    "s_mov_b32 s54, s27\n\t"
    "s_mov_b32 s55, 0x20000\n\t"
    "s_lshl_b32 s26, s23, 20\n\t"
    "s_mov_b64 s[72:73], %6\n\t"
    "s_add_u32 s72, s72, s26\n\t"
    "s_addc_u32 s73, s73, 0\n\t"
    "s_mov_b32 s56, s72\n\t"
    "s_and_b32 s57, s73, 0xffff\n\t"
    "s_mov_b32 s58, 0x100000\n\t"
    "s_mov_b32 s59, 0x20000\n\t"
    "s_lshl_b32 s26, s23, 16\n\t"
    "s_mov_b64 s[72:73], %5\n\t"
    "s_add_u32 s72, s72, s26\n\t"
    "s_addc_u32 s73, s73, 0\n\t"
    "s_mov_b32 s68, s72\n\t"
    "s_and_b32 s69, s73, 0xffff\n\t"
    "s_mov_b32 s70, 0x10000\n\t"
    "s_mov_b32 s71, 0x20000\n\t"
    "s_mov_b64 s[92:93], %6\n\t"
    "s_add_u32 s92, s92, 0x1000000\n\t"
    "s_addc_u32 s93, s93, 0\n\t"
    "v_mfma_f32_32x32x16_bf16 v[16:31], a[128:131], v[128:131], 0\n\t"
    "ds_read_b64_tr_b16 a[160:161], v116 offset:16384\n\t"
    "ds_read_b64_tr_b16 a[162:163], v116 offset:17408\n\t"
    "ds_read_b64_tr_b16 a[164:165], v117 offset:16384\n\t"
    "ds_read_b64_tr_b16 a[166:167], v117 offset:17408\n\t"
    "s_add_u32 m0, s75, 98304\n\t"
    "s_add_u32 s34, s84, 1\n\t"
    "s_and_b32 s34, s34, 3\n\t"
    "s_lshl_b32 s34, s34, 8\n\t"
    "s_add_u32 s34, s34, s35\n\t"
    "buffer_load_dwordx4 v12, s[36:39], s34 offen lds\n\t"
    "v_mfma_f32_32x32x16_bf16 v[32:47], a[132:135], v[128:131], 0\n\t"
    "ds_read_b64_tr_b16 a[168:169], v118 offset:16384\n\t"
    "ds_read_b64_tr_b16 a[170:171], v118 offset:17408\n\t"
    "ds_read_b64_tr_b16 a[172:173], v119 offset:16384\n\t"
    "ds_read_b64_tr_b16 a[174:175], v119 offset:17408\n\t"
    "buffer_load_dwordx4 v13, s[36:39], s34 offen offset:1024 lds\n\t"
    "v_mfma_f32_32x32x16_bf16 v[48:63], a[136:139], v[128:131], 0\n\t"
    "ds_read_b64_tr_b16 a[176:177], v116 offset:20480\n\t"
    "ds_read_b64_tr_b16 a[178:179], v116 offset:21504\n\t"
    "ds_read_b64_tr_b16 a[180:181], v117 offset:20480\n\t"
    "ds_read_b64_tr_b16 a[182:183], v117 offset:21504\n\t"
    "buffer_load_dwordx4 v14, s[36:39], s34 offen offset:2048 lds\n\t"
    "v_mfma_f32_32x32x16_bf16 v[64:79], a[140:143], v[128:131], 0\n\t"
    "ds_read_b64_tr_b16 a[184:185], v118 offset:20480\n\t"
    "ds_read_b64_tr_b16 a[186:187], v118 offset:21504\n\t"
    "ds_read_b64_tr_b16 a[188:189], v119 offset:20480\n\t"
    "ds_read_b64_tr_b16 a[190:191], v119 offset:21504\n\t"
    "buffer_load_dwordx4 v15, s[36:39], s34 offen offset:3072 lds\n\t"
    "v_mfma_f32_32x32x16_bf16 v[16:31], a[144:147], v[132:135], v[16:31]\n\t"
    "s_add_u32 m0, s28, 0\n\t"
    "s_add_u32 s85, s74, 0\n\t"
    "buffer_load_dwordx4 v111, s[40:43], s85 offen lds\n\t"
    "v_mfma_f32_32x32x16_bf16 v[32:47], a[148:151], v[132:135], v[32:47]\n\t"
    "buffer_load_dwordx4 v112, s[40:43], s85 offen offset:1024 lds\n\t"
    "v_mfma_f32_32x32x16_bf16 v[48:63], a[152:155], v[132:135], v[48:63]\n\t"
    "s_add_u32 m0, s28, 4096\n\t"
    "s_add_u32 s85, s74, 131072\n\t"
    "buffer_load_dwordx4 v111, s[40:43], s85 offen lds\n\t"
    "v_mfma_f32_32x32x16_bf16 v[64:79], a[156:159], v[132:135], v[64:79]\n\t"
    "buffer_load_dwordx4 v112, s[40:43], s85 offen offset:1024 lds\n\t"
    "s_add_u32 s74, s74, 262144\n\t"
    "s_and_b32 s74, s74, 4194303\n\t"
    "s_waitcnt vmcnt(20) lgkmcnt(0)\n\t"
    "s_barrier\n\t"
    "v_lshl_add_u32 v9, v3, 2, v6\n\t"
    "v_lshlrev_b32 v9, 8, v9\n\t"
    "v_lshrrev_b32 v10, 1, v7\n\t"
    "v_lshl_add_u32 v10, v8, 1, v10\n\t"
    "v_lshl_add_u32 v9, v10, 4, v9\n\t"
    "v_and_b32 v10, 1, v7\n\t"
    "v_lshl_add_u32 v9, v10, 3, v9\n\t"
    "v_xor_b32 v10, 0, v6\n\t"
    "v_lshl_add_u32 v98, v10, 6, v9\n\t"
    "v_add_u32 v102, 65536, v98\n\t"
    "v_xor_b32 v10, 1, v6\n\t"
    "v_lshl_add_u32 v99, v10, 6, v9\n\t"
    "v_add_u32 v103, 65536, v99\n\t"
    "v_xor_b32 v10, 2, v6\n\t"
    "v_lshl_add_u32 v100, v10, 6, v9\n\t"
    "v_add_u32 v104, 65536, v100\n\t"
    "v_xor_b32 v10, 3, v6\n\t"
    "v_lshl_add_u32 v101, v10, 6, v9\n\t"
    "v_add_u32 v105, 65536, v101\n\t"
    "v_add_u32 v9, 0, v5\n\t"
    "v_xor_b32 v9, v9, v4\n\t"
    "v_lshlrev_b32 v9, 4, v9\n\t"
    "v_lshl_add_u32 v106, v5, 8, v9\n\t"
    "v_add_u32 v9, 4, v5\n\t"
    "v_xor_b32 v9, v9, v4\n\t"
    "v_lshlrev_b32 v9, 4, v9\n\t"
    "v_lshl_add_u32 v107, v5, 8, v9\n\t"
    "v_add_u32 v9, 8, v5\n\t"
    "v_xor_b32 v9, v9, v4\n\t"
    "v_lshlrev_b32 v9, 4, v9\n\t"
    "v_lshl_add_u32 v108, v5, 8, v9\n\t"
    "v_add_u32 v9, 12, v5\n\t"
    "v_xor_b32 v9, v9, v4\n\t"
    "v_lshlrev_b32 v9, 4, v9\n\t"
    "v_lshl_add_u32 v109, v5, 8, v9\n\t"
    "v_lshlrev_b32 v9, 2, v5\n\t"
    "v_xor_b32 v9, v9, v4\n\t"
    "v_lshlrev_b32 v9, 4, v9\n\t"
    "v_lshl_add_u32 v110, v5, 8, v9\n\t"
    "s_and_b32 s26, s21, 1\n\t"
    "s_lshl_b32 s26, s26, 13\n\t"
    "v_lshlrev_b32 v9, 7, v1\n\t"
    "v_add_u32 v121, s26, v9\n\t"
    "s_lshl_b32 s81, s24, 14\n\t"
    "v_mov_b32 v9, 65536\n\t"
    "global_load_dwordx2 v[114:115], v9, s[92:93] sc1\n\t"
    "v_mfma_f32_32x32x16_bf16 v[16:31], a[160:163], v[136:139], v[16:31]\n\t"
    "ds_read_b64_tr_b16 a[128:129], v116 offset:32768\n\t"
    "ds_read_b64_tr_b16 a[130:131], v116 offset:33792\n\t"
    "ds_read_b64_tr_b16 a[132:133], v117 offset:32768\n\t"
    "ds_read_b64_tr_b16 a[134:135], v117 offset:33792\n\t"
    "s_add_u32 m0, s28, 16384\n\t"
    "s_add_u32 s85, s74, 0\n\t"
    "buffer_load_dwordx4 v111, s[40:43], s85 offen lds\n\t"
    "v_mfma_f32_32x32x16_bf16 v[32:47], a[164:167], v[136:139], v[32:47]\n\t"
    "ds_read_b64_tr_b16 a[136:137], v118 offset:32768\n\t"
    "ds_read_b64_tr_b16 a[138:139], v118 offset:33792\n\t"
    "ds_read_b64_tr_b16 a[140:141], v119 offset:32768\n\t"
    "ds_read_b64_tr_b16 a[142:143], v119 offset:33792\n\t"
    "buffer_load_dwordx4 v112, s[40:43], s85 offen offset:1024 lds\n\t"
    "v_mfma_f32_32x32x16_bf16 v[48:63], a[168:171], v[136:139], v[48:63]\n\t"
    "ds_read_b64_tr_b16 a[144:145], v116 offset:36864\n\t"
    "ds_read_b64_tr_b16 a[146:147], v116 offset:37888\n\t"
    "ds_read_b64_tr_b16 a[148:149], v117 offset:36864\n\t"
    "ds_read_b64_tr_b16 a[150:151], v117 offset:37888\n\t"
    "s_add_u32 m0, s28, 20480\n\t"
    "s_add_u32 s85, s74, 131072\n\t"
    "buffer_load_dwordx4 v111, s[40:43], s85 offen lds\n\t"
    "v_mfma_f32_32x32x16_bf16 v[64:79], a[172:175], v[136:139], v[64:79]\n\t"
    "ds_read_b64_tr_b16 a[152:153], v118 offset:36864\n\t"
    "ds_read_b64_tr_b16 a[154:155], v118 offset:37888\n\t"
    "ds_read_b64_tr_b16 a[156:157], v119 offset:36864\n\t"
    "ds_read_b64_tr_b16 a[158:159], v119 offset:37888\n\t"
    "buffer_load_dwordx4 v112, s[40:43], s85 offen offset:1024 lds\n\t"
    "s_add_u32 s74, s74, 262144\n\t"
    "s_and_b32 s74, s74, 4194303\n\t"
    "v_mfma_f32_32x32x16_bf16 v[16:31], a[176:179], v[140:143], v[16:31]\n\t"
    "v_mfma_f32_32x32x16_bf16 v[32:47], a[180:183], v[140:143], v[32:47]\n\t"
    "v_mfma_f32_32x32x16_bf16 v[48:63], a[184:187], v[140:143], v[48:63]\n\t"
    "v_mfma_f32_32x32x16_bf16 v[64:79], a[188:191], v[140:143], v[64:79]\n\t"
    "s_waitcnt vmcnt(21) lgkmcnt(0)\n\t"
    "s_barrier\n\t"
    "v_mfma_f32_32x32x16_bf16 v[16:31], a[128:131], v[144:147], v[16:31]\n\t"
    "ds_read_b64_tr_b16 a[160:161], v116 offset:49152\n\t"
    "ds_read_b64_tr_b16 a[162:163], v116 offset:50176\n\t"
    "ds_read_b64_tr_b16 a[164:165], v117 offset:49152\n\t"
    "ds_read_b64_tr_b16 a[166:167], v117 offset:50176\n\t"
    "s_add_u32 m0, s28, 32768\n\t"
    "s_add_u32 s85, s74, 0\n\t"
    "buffer_load_dwordx4 v111, s[40:43], s85 offen lds\n\t"
    "v_mfma_f32_32x32x16_bf16 v[32:47], a[132:135], v[144:147], v[32:47]\n\t"
    "ds_read_b64_tr_b16 a[168:169], v118 offset:49152\n\t"
    "ds_read_b64_tr_b16 a[170:171], v118 offset:50176\n\t"
    "ds_read_b64_tr_b16 a[172:173], v119 offset:49152\n\t"
    "ds_read_b64_tr_b16 a[174:175], v119 offset:50176\n\t"
    "buffer_load_dwordx4 v112, s[40:43], s85 offen offset:1024 lds\n\t"
    "v_mfma_f32_32x32x16_bf16 v[48:63], a[136:139], v[144:147], v[48:63]\n\t"
    "ds_read_b64_tr_b16 a[176:177], v116 offset:53248\n\t"
    "ds_read_b64_tr_b16 a[178:179], v116 offset:54272\n\t"
    "ds_read_b64_tr_b16 a[180:181], v117 offset:53248\n\t"
    "ds_read_b64_tr_b16 a[182:183], v117 offset:54272\n\t"
    "s_add_u32 m0, s28, 36864\n\t"
    "s_add_u32 s85, s74, 131072\n\t"
    "buffer_load_dwordx4 v111, s[40:43], s85 offen lds\n\t"
    "v_mfma_f32_32x32x16_bf16 v[64:79], a[140:143], v[144:147], v[64:79]\n\t"
    "ds_read_b64_tr_b16 a[184:185], v118 offset:53248\n\t"
    "ds_read_b64_tr_b16 a[186:187], v118 offset:54272\n\t"
    "ds_read_b64_tr_b16 a[188:189], v119 offset:53248\n\t"
    "ds_read_b64_tr_b16 a[190:191], v119 offset:54272\n\t"
    "buffer_load_dwordx4 v112, s[40:43], s85 offen offset:1024 lds\n\t"
    "s_add_u32 s74, s74, 262144\n\t"
    "s_and_b32 s74, s74, 4194303\n\t"
    "v_mfma_f32_32x32x16_bf16 v[16:31], a[144:147], v[148:151], v[16:31]\n\t"
    "v_mfma_f32_32x32x16_bf16 v[32:47], a[148:151], v[148:151], v[32:47]\n\t"
    "v_mfma_f32_32x32x16_bf16 v[48:63], a[152:155], v[148:151], v[48:63]\n\t"
    "v_mfma_f32_32x32x16_bf16 v[64:79], a[156:159], v[148:151], v[64:79]\n\t"
    "s_waitcnt vmcnt(13) lgkmcnt(0)\n\t"
    "s_barrier\n\t"
    "s_lshl_b32 s26, s83, 13\n\t"
    "s_add_u32 s26, s26, 98304\n\t"
    "v_add_u32 v9, s26, v82\n\t"
    "ds_read_b128 v[160:163], v9\n\t"
    "v_add_u32 v10, s26, v83\n\t"
    "ds_read_b128 v[164:167], v10\n\t"
    "v_add_u32 v11, s26, v84\n\t"
    "ds_read_b128 v[168:171], v11\n\t"
    "v_add_u32 v9, s26, v85\n\t"
    "ds_read_b128 v[172:175], v9\n\t"
    "v_add_u32 v10, s26, v86\n\t"
    "ds_read_b128 v[176:179], v10\n\t"
    "v_add_u32 v11, s26, v87\n\t"
    "ds_read_b128 v[180:183], v11\n\t"
    "v_add_u32 v9, s26, v88\n\t"
    "ds_read_b128 v[184:187], v9\n\t"
    "v_add_u32 v10, s26, v89\n\t"
    "ds_read_b128 v[188:191], v10\n\t"
    "v_mfma_f32_32x32x16_bf16 v[16:31], a[160:163], v[152:155], v[16:31]\n\t"
    "ds_read_b64_tr_b16 a[128:129], v124 offset:16384\n\t"
    "ds_read_b64_tr_b16 a[130:131], v124 offset:17408\n\t"
    "ds_read_b64_tr_b16 a[132:133], v125 offset:16384\n\t"
    "ds_read_b64_tr_b16 a[134:135], v125 offset:17408\n\t"
    "s_add_u32 m0, s28, 49152\n\t"
    "s_add_u32 s85, s74, 0\n\t"
    "buffer_load_dwordx4 v111, s[40:43], s85 offen lds\n\t"
    "v_mfma_f32_32x32x16_bf16 v[32:47], a[164:167], v[152:155], v[32:47]\n\t"
    "ds_read_b64_tr_b16 a[136:137], v126 offset:16384\n\t"
    "ds_read_b64_tr_b16 a[138:139], v126 offset:17408\n\t"
    "ds_read_b64_tr_b16 a[140:141], v127 offset:16384\n\t"
    "ds_read_b64_tr_b16 a[142:143], v127 offset:17408\n\t"
    "buffer_load_dwordx4 v112, s[40:43], s85 offen offset:1024 lds\n\t"
    "v_mfma_f32_32x32x16_bf16 v[48:63], a[168:171], v[152:155], v[48:63]\n\t"
    "ds_read_b64_tr_b16 a[144:145], v124 offset:20480\n\t"
    "ds_read_b64_tr_b16 a[146:147], v124 offset:21504\n\t"
    "ds_read_b64_tr_b16 a[148:149], v125 offset:20480\n\t"
    "ds_read_b64_tr_b16 a[150:151], v125 offset:21504\n\t"
    "s_add_u32 m0, s28, 53248\n\t"
    "s_add_u32 s85, s74, 131072\n\t"
    "buffer_load_dwordx4 v111, s[40:43], s85 offen lds\n\t"
    "v_mfma_f32_32x32x16_bf16 v[64:79], a[172:175], v[152:155], v[64:79]\n\t"
    "ds_read_b64_tr_b16 a[152:153], v126 offset:20480\n\t"
    "ds_read_b64_tr_b16 a[154:155], v126 offset:21504\n\t"
    "ds_read_b64_tr_b16 a[156:157], v127 offset:20480\n\t"
    "ds_read_b64_tr_b16 a[158:159], v127 offset:21504\n\t"
    "buffer_load_dwordx4 v112, s[40:43], s85 offen offset:1024 lds\n\t"
    "s_add_u32 s74, s74, 262144\n\t"
    "s_and_b32 s74, s74, 4194303\n\t"
    "v_mfma_f32_32x32x16_bf16 v[16:31], a[176:179], v[156:159], v[16:31]\n\t"
    "v_mfma_f32_32x32x16_bf16 v[32:47], a[180:183], v[156:159], v[32:47]\n\t"
    "v_mfma_f32_32x32x16_bf16 v[48:63], a[184:187], v[156:159], v[48:63]\n\t"
    "v_mfma_f32_32x32x16_bf16 v[64:79], a[188:191], v[156:159], v[64:79]\n\t"
    "s_waitcnt lgkmcnt(0)\n\t"
    "s_barrier\n\t"
    "v_mfma_f32_32x32x16_bf16 v[16:31], a[128:131], v[160:163], v[16:31]\n\t"
    "ds_read_b64_tr_b16 a[160:161], v124 offset:32768\n\t"
    "ds_read_b64_tr_b16 a[162:163], v124 offset:33792\n\t"
    "ds_read_b64_tr_b16 a[164:165], v125 offset:32768\n\t"
    "ds_read_b64_tr_b16 a[166:167], v125 offset:33792\n\t"
    "s_add_u32 m0, s75, 98304\n\t"
    "s_add_u32 s34, s84, 2\n\t"
    "s_and_b32 s34, s34, 3\n\t"
    "s_lshl_b32 s34, s34, 8\n\t"
    "s_add_u32 s34, s34, s35\n\t"
    "buffer_load_dwordx4 v12, s[36:39], s34 offen lds\n\t"
    "v_mfma_f32_32x32x16_bf16 v[32:47], a[132:135], v[160:163], v[32:47]\n\t"
    "ds_read_b64_tr_b16 a[168:169], v126 offset:32768\n\t"
    "ds_read_b64_tr_b16 a[170:171], v126 offset:33792\n\t"
    "ds_read_b64_tr_b16 a[172:173], v127 offset:32768\n\t"
    "ds_read_b64_tr_b16 a[174:175], v127 offset:33792\n\t"
    "buffer_load_dwordx4 v13, s[36:39], s34 offen offset:1024 lds\n\t"
    "v_mfma_f32_32x32x16_bf16 v[48:63], a[136:139], v[160:163], v[48:63]\n\t"
    "ds_read_b64_tr_b16 a[176:177], v124 offset:36864\n\t"
    "ds_read_b64_tr_b16 a[178:179], v124 offset:37888\n\t"
    "ds_read_b64_tr_b16 a[180:181], v125 offset:36864\n\t"
    "ds_read_b64_tr_b16 a[182:183], v125 offset:37888\n\t"
    "buffer_load_dwordx4 v14, s[36:39], s34 offen offset:2048 lds\n\t"
    "v_mfma_f32_32x32x16_bf16 v[64:79], a[140:143], v[160:163], v[64:79]\n\t"
    "ds_read_b64_tr_b16 a[184:185], v126 offset:36864\n\t"
    "ds_read_b64_tr_b16 a[186:187], v126 offset:37888\n\t"
    "ds_read_b64_tr_b16 a[188:189], v127 offset:36864\n\t"
    "ds_read_b64_tr_b16 a[190:191], v127 offset:37888\n\t"
    "buffer_load_dwordx4 v15, s[36:39], s34 offen offset:3072 lds\n\t"
    "v_mfma_f32_32x32x16_bf16 v[16:31], a[144:147], v[164:167], v[16:31]\n\t"
    "s_add_u32 m0, s28, 81920\n\t"
    "s_add_u32 s85, s74, 0\n\t"
    "buffer_load_dwordx4 v111, s[40:43], s85 offen lds\n\t"
    "v_mfma_f32_32x32x16_bf16 v[32:47], a[148:151], v[164:167], v[32:47]\n\t"
    "buffer_load_dwordx4 v112, s[40:43], s85 offen offset:1024 lds\n\t"
    "v_mfma_f32_32x32x16_bf16 v[48:63], a[152:155], v[164:167], v[48:63]\n\t"
    "s_add_u32 m0, s28, 86016\n\t"
    "s_add_u32 s85, s74, 131072\n\t"
    "buffer_load_dwordx4 v111, s[40:43], s85 offen lds\n\t"
    "v_mfma_f32_32x32x16_bf16 v[64:79], a[156:159], v[164:167], v[64:79]\n\t"
    "buffer_load_dwordx4 v112, s[40:43], s85 offen offset:1024 lds\n\t"
    "s_add_u32 s74, s74, 262144\n\t"
    "s_and_b32 s74, s74, 4194303\n\t"
    "s_waitcnt vmcnt(21) lgkmcnt(0)\n\t"
    "s_barrier\n\t"
    "v_mfma_f32_32x32x16_bf16 v[16:31], a[160:163], v[168:171], v[16:31]\n\t"
    "ds_read_b64_tr_b16 a[128:129], v116\n\t"
    "ds_read_b64_tr_b16 a[130:131], v116 offset:1024\n\t"
    "ds_read_b64_tr_b16 a[132:133], v117\n\t"
    "ds_read_b64_tr_b16 a[134:135], v117 offset:1024\n\t"
    "s_add_u32 m0, s28, 98304\n\t"
    "s_add_u32 s85, s74, 0\n\t"
    "buffer_load_dwordx4 v111, s[40:43], s85 offen lds\n\t"
    "v_mfma_f32_32x32x16_bf16 v[32:47], a[164:167], v[168:171], v[32:47]\n\t"
    "ds_read_b64_tr_b16 a[136:137], v118\n\t"
    "ds_read_b64_tr_b16 a[138:139], v118 offset:1024\n\t"
    "ds_read_b64_tr_b16 a[140:141], v119\n\t"
    "ds_read_b64_tr_b16 a[142:143], v119 offset:1024\n\t"
    "buffer_load_dwordx4 v112, s[40:43], s85 offen offset:1024 lds\n\t"
    "v_mfma_f32_32x32x16_bf16 v[48:63], a[168:171], v[168:171], v[48:63]\n\t"
    "ds_read_b64_tr_b16 a[144:145], v116 offset:4096\n\t"
    "ds_read_b64_tr_b16 a[146:147], v116 offset:5120\n\t"
    "ds_read_b64_tr_b16 a[148:149], v117 offset:4096\n\t"
    "ds_read_b64_tr_b16 a[150:151], v117 offset:5120\n\t"
    "s_add_u32 m0, s28, 102400\n\t"
    "s_add_u32 s85, s74, 131072\n\t"
    "buffer_load_dwordx4 v111, s[40:43], s85 offen lds\n\t"
    "v_mfma_f32_32x32x16_bf16 v[64:79], a[172:175], v[168:171], v[64:79]\n\t"
    "ds_read_b64_tr_b16 a[152:153], v118 offset:4096\n\t"
    "ds_read_b64_tr_b16 a[154:155], v118 offset:5120\n\t"
    "ds_read_b64_tr_b16 a[156:157], v119 offset:4096\n\t"
    "ds_read_b64_tr_b16 a[158:159], v119 offset:5120\n\t"
    "buffer_load_dwordx4 v112, s[40:43], s85 offen offset:1024 lds\n\t"
    "s_add_u32 s74, s74, 262144\n\t"
    "s_and_b32 s74, s74, 4194303\n\t"
    "v_mfma_f32_32x32x16_bf16 v[16:31], a[176:179], v[172:175], v[16:31]\n\t"
    "v_mfma_f32_32x32x16_bf16 v[32:47], a[180:183], v[172:175], v[32:47]\n\t"
    "v_mfma_f32_32x32x16_bf16 v[48:63], a[184:187], v[172:175], v[48:63]\n\t"
    "v_mfma_f32_32x32x16_bf16 v[64:79], a[188:191], v[172:175], v[64:79]\n\t"
    "s_waitcnt vmcnt(20) lgkmcnt(0)\n\t"
    "s_barrier\n\t"
    "v_mfma_f32_32x32x16_bf16 v[16:31], a[128:131], v[176:179], v[16:31]\n\t"
    "ds_read_b64_tr_b16 a[160:161], v116 offset:16384\n\t"
    "ds_read_b64_tr_b16 a[162:163], v116 offset:17408\n\t"
    "ds_read_b64_tr_b16 a[164:165], v117 offset:16384\n\t"
    "ds_read_b64_tr_b16 a[166:167], v117 offset:17408\n\t"
    "s_add_u32 m0, s28, 0\n\t"
    "s_add_u32 s85, s74, 0\n\t"
    "buffer_load_dwordx4 v111, s[40:43], s85 offen lds\n\t"
    "v_mfma_f32_32x32x16_bf16 v[32:47], a[132:135], v[176:179], v[32:47]\n\t"
    "ds_read_b64_tr_b16 a[168:169], v118 offset:16384\n\t"
    "ds_read_b64_tr_b16 a[170:171], v118 offset:17408\n\t"
    "ds_read_b64_tr_b16 a[172:173], v119 offset:16384\n\t"
    "ds_read_b64_tr_b16 a[174:175], v119 offset:17408\n\t"
    "buffer_load_dwordx4 v112, s[40:43], s85 offen offset:1024 lds\n\t"
    "v_mfma_f32_32x32x16_bf16 v[48:63], a[136:139], v[176:179], v[48:63]\n\t"
    "ds_read_b64_tr_b16 a[176:177], v116 offset:20480\n\t"
    "ds_read_b64_tr_b16 a[178:179], v116 offset:21504\n\t"
    "ds_read_b64_tr_b16 a[180:181], v117 offset:20480\n\t"
    "ds_read_b64_tr_b16 a[182:183], v117 offset:21504\n\t"
    "s_add_u32 m0, s28, 4096\n\t"
    "s_add_u32 s85, s74, 131072\n\t"
    "buffer_load_dwordx4 v111, s[40:43], s85 offen lds\n\t"
    "v_mfma_f32_32x32x16_bf16 v[64:79], a[140:143], v[176:179], v[64:79]\n\t"
    "ds_read_b64_tr_b16 a[184:185], v118 offset:20480\n\t"
    "ds_read_b64_tr_b16 a[186:187], v118 offset:21504\n\t"
    "ds_read_b64_tr_b16 a[188:189], v119 offset:20480\n\t"
    "ds_read_b64_tr_b16 a[190:191], v119 offset:21504\n\t"
    "buffer_load_dwordx4 v112, s[40:43], s85 offen offset:1024 lds\n\t"
    "s_add_u32 s74, s74, 262144\n\t"
    "s_and_b32 s74, s74, 4194303\n\t"
    "v_mfma_f32_32x32x16_bf16 v[16:31], a[144:147], v[180:183], v[16:31]\n\t"
    "v_mfma_f32_32x32x16_bf16 v[32:47], a[148:151], v[180:183], v[32:47]\n\t"
    "v_mfma_f32_32x32x16_bf16 v[48:63], a[152:155], v[180:183], v[48:63]\n\t"
    "v_mfma_f32_32x32x16_bf16 v[64:79], a[156:159], v[180:183], v[64:79]\n\t"
    "s_waitcnt vmcnt(12) lgkmcnt(0)\n\t"
    "s_barrier\n\t"
    "s_lshl_b32 s26, s83, 13\n\t"
    "s_add_u32 s26, s26, 98304\n\t"
    "v_add_u32 v9, s26, v82\n\t"
    "ds_read_b128 v[192:195], v9\n\t"
    "v_add_u32 v10, s26, v83\n\t"
    "ds_read_b128 v[196:199], v10\n\t"
    "v_add_u32 v11, s26, v84\n\t"
    "ds_read_b128 v[200:203], v11\n\t"
    "v_add_u32 v9, s26, v85\n\t"
    "ds_read_b128 v[204:207], v9\n\t"
    "v_add_u32 v10, s26, v86\n\t"
    "ds_read_b128 v[208:211], v10\n\t"
    "v_add_u32 v11, s26, v87\n\t"
    "ds_read_b128 v[212:215], v11\n\t"
    "v_add_u32 v9, s26, v88\n\t"
    "ds_read_b128 v[216:219], v9\n\t"
    "v_add_u32 v10, s26, v89\n\t"
    "ds_read_b128 v[220:223], v10\n\t"
    "v_mfma_f32_32x32x16_bf16 v[16:31], a[160:163], v[184:187], v[16:31]\n\t"
    "ds_read_b64_tr_b16 a[128:129], v116 offset:32768\n\t"
    "ds_read_b64_tr_b16 a[130:131], v116 offset:33792\n\t"
    "ds_read_b64_tr_b16 a[132:133], v117 offset:32768\n\t"
    "ds_read_b64_tr_b16 a[134:135], v117 offset:33792\n\t"
    "s_add_u32 m0, s28, 16384\n\t"
    "s_add_u32 s85, s74, 0\n\t"
    "buffer_load_dwordx4 v111, s[40:43], s85 offen lds\n\t"
    "v_mfma_f32_32x32x16_bf16 v[32:47], a[164:167], v[184:187], v[32:47]\n\t"
    "ds_read_b64_tr_b16 a[136:137], v118 offset:32768\n\t"
    "ds_read_b64_tr_b16 a[138:139], v118 offset:33792\n\t"
    "ds_read_b64_tr_b16 a[140:141], v119 offset:32768\n\t"
    "ds_read_b64_tr_b16 a[142:143], v119 offset:33792\n\t"
    "buffer_load_dwordx4 v112, s[40:43], s85 offen offset:1024 lds\n\t"
    "v_mfma_f32_32x32x16_bf16 v[48:63], a[168:171], v[184:187], v[48:63]\n\t"
    "ds_read_b64_tr_b16 a[144:145], v116 offset:36864\n\t"
    "ds_read_b64_tr_b16 a[146:147], v116 offset:37888\n\t"
    "ds_read_b64_tr_b16 a[148:149], v117 offset:36864\n\t"
    "ds_read_b64_tr_b16 a[150:151], v117 offset:37888\n\t"
    "s_add_u32 m0, s28, 20480\n\t"
    "s_add_u32 s85, s74, 131072\n\t"
    "buffer_load_dwordx4 v111, s[40:43], s85 offen lds\n\t"
    "v_mfma_f32_32x32x16_bf16 v[64:79], a[172:175], v[184:187], v[64:79]\n\t"
    "ds_read_b64_tr_b16 a[152:153], v118 offset:36864\n\t"
    "ds_read_b64_tr_b16 a[154:155], v118 offset:37888\n\t"
    "ds_read_b64_tr_b16 a[156:157], v119 offset:36864\n\t"
    "ds_read_b64_tr_b16 a[158:159], v119 offset:37888\n\t"
    "buffer_load_dwordx4 v112, s[40:43], s85 offen offset:1024 lds\n\t"
    "s_add_u32 s74, s74, 262144\n\t"
    "s_and_b32 s74, s74, 4194303\n\t"
    "v_mfma_f32_32x32x16_bf16 v[16:31], a[176:179], v[188:191], v[16:31]\n\t"
    "v_mfma_f32_32x32x16_bf16 v[32:47], a[180:183], v[188:191], v[32:47]\n\t"
    "v_mfma_f32_32x32x16_bf16 v[48:63], a[184:187], v[188:191], v[48:63]\n\t"
    "v_mfma_f32_32x32x16_bf16 v[64:79], a[188:191], v[188:191], v[64:79]\n\t"
    "s_waitcnt lgkmcnt(0)\n\t"
    "s_barrier\n\t"
    "v_mfma_f32_32x32x16_bf16 v[16:31], a[128:131], v[192:195], v[16:31]\n\t"
    "ds_read_b64_tr_b16 a[160:161], v116 offset:49152\n\t"
    "ds_read_b64_tr_b16 a[162:163], v116 offset:50176\n\t"
    "ds_read_b64_tr_b16 a[164:165], v117 offset:49152\n\t"
    "ds_read_b64_tr_b16 a[166:167], v117 offset:50176\n\t"
    "s_add_u32 m0, s75, 98304\n\t"
    "s_add_u32 s34, s84, 3\n\t"
    "s_and_b32 s34, s34, 3\n\t"
    "s_lshl_b32 s34, s34, 8\n\t"
    "s_add_u32 s34, s34, s35\n\t"
    "buffer_load_dwordx4 v12, s[36:39], s34 offen lds\n\t"
    "v_mfma_f32_32x32x16_bf16 v[32:47], a[132:135], v[192:195], v[32:47]\n\t"
    "ds_read_b64_tr_b16 a[168:169], v118 offset:49152\n\t"
    "ds_read_b64_tr_b16 a[170:171], v118 offset:50176\n\t"
    "ds_read_b64_tr_b16 a[172:173], v119 offset:49152\n\t"
    "ds_read_b64_tr_b16 a[174:175], v119 offset:50176\n\t"
    "buffer_load_dwordx4 v13, s[36:39], s34 offen offset:1024 lds\n\t"
    "v_mfma_f32_32x32x16_bf16 v[48:63], a[136:139], v[192:195], v[48:63]\n\t"
    "ds_read_b64_tr_b16 a[176:177], v116 offset:53248\n\t"
    "ds_read_b64_tr_b16 a[178:179], v116 offset:54272\n\t"
    "ds_read_b64_tr_b16 a[180:181], v117 offset:53248\n\t"
    "ds_read_b64_tr_b16 a[182:183], v117 offset:54272\n\t"
    "buffer_load_dwordx4 v14, s[36:39], s34 offen offset:2048 lds\n\t"
    "v_mfma_f32_32x32x16_bf16 v[64:79], a[140:143], v[192:195], v[64:79]\n\t"
    "ds_read_b64_tr_b16 a[184:185], v118 offset:53248\n\t"
    "ds_read_b64_tr_b16 a[186:187], v118 offset:54272\n\t"
    "ds_read_b64_tr_b16 a[188:189], v119 offset:53248\n\t"
    "ds_read_b64_tr_b16 a[190:191], v119 offset:54272\n\t"
    "buffer_load_dwordx4 v15, s[36:39], s34 offen offset:3072 lds\n\t"
    "v_mfma_f32_32x32x16_bf16 v[16:31], a[144:147], v[196:199], v[16:31]\n\t"
    "s_add_u32 m0, s28, 32768\n\t"
    "s_add_u32 s85, s74, 0\n\t"
    "buffer_load_dwordx4 v111, s[40:43], s85 offen lds\n\t"
    "v_mfma_f32_32x32x16_bf16 v[32:47], a[148:151], v[196:199], v[32:47]\n\t"
    "buffer_load_dwordx4 v112, s[40:43], s85 offen offset:1024 lds\n\t"
    "v_mfma_f32_32x32x16_bf16 v[48:63], a[152:155], v[196:199], v[48:63]\n\t"
    "s_add_u32 m0, s28, 36864\n\t"
    "s_add_u32 s85, s74, 131072\n\t"
    "buffer_load_dwordx4 v111, s[40:43], s85 offen lds\n\t"
    "v_mfma_f32_32x32x16_bf16 v[64:79], a[156:159], v[196:199], v[64:79]\n\t"
    "buffer_load_dwordx4 v112, s[40:43], s85 offen offset:1024 lds\n\t"
    "s_add_u32 s74, s74, 262144\n\t"
    "s_and_b32 s74, s74, 4194303\n\t"
    "s_waitcnt vmcnt(20) lgkmcnt(0)\n\t"
    "s_barrier\n\t"
    "v_mfma_f32_32x32x16_bf16 v[16:31], a[160:163], v[200:203], v[16:31]\n\t"
    "ds_read_b64_tr_b16 a[128:129], v124 offset:16384\n\t"
    "ds_read_b64_tr_b16 a[130:131], v124 offset:17408\n\t"
    "ds_read_b64_tr_b16 a[132:133], v125 offset:16384\n\t"
    "ds_read_b64_tr_b16 a[134:135], v125 offset:17408\n\t"
    "s_add_u32 m0, s28, 49152\n\t"
    "s_add_u32 s85, s74, 0\n\t"
    "buffer_load_dwordx4 v111, s[40:43], s85 offen lds\n\t"
    "v_mfma_f32_32x32x16_bf16 v[32:47], a[164:167], v[200:203], v[32:47]\n\t"
    "ds_read_b64_tr_b16 a[136:137], v126 offset:16384\n\t"
    "ds_read_b64_tr_b16 a[138:139], v126 offset:17408\n\t"
    "ds_read_b64_tr_b16 a[140:141], v127 offset:16384\n\t"
    "ds_read_b64_tr_b16 a[142:143], v127 offset:17408\n\t"
    "buffer_load_dwordx4 v112, s[40:43], s85 offen offset:1024 lds\n\t"
    "v_mfma_f32_32x32x16_bf16 v[48:63], a[168:171], v[200:203], v[48:63]\n\t"
    "ds_read_b64_tr_b16 a[144:145], v124 offset:20480\n\t"
    "ds_read_b64_tr_b16 a[146:147], v124 offset:21504\n\t"
    "ds_read_b64_tr_b16 a[148:149], v125 offset:20480\n\t"
    "ds_read_b64_tr_b16 a[150:151], v125 offset:21504\n\t"
    "s_add_u32 m0, s28, 53248\n\t"
    "s_add_u32 s85, s74, 131072\n\t"
    "buffer_load_dwordx4 v111, s[40:43], s85 offen lds\n\t"
    "v_mfma_f32_32x32x16_bf16 v[64:79], a[172:175], v[200:203], v[64:79]\n\t"
    "ds_read_b64_tr_b16 a[152:153], v126 offset:20480\n\t"
    "ds_read_b64_tr_b16 a[154:155], v126 offset:21504\n\t"
    "ds_read_b64_tr_b16 a[156:157], v127 offset:20480\n\t"
    "ds_read_b64_tr_b16 a[158:159], v127 offset:21504\n\t"
    "buffer_load_dwordx4 v112, s[40:43], s85 offen offset:1024 lds\n\t"
    "s_add_u32 s74, s74, 262144\n\t"
    "s_and_b32 s74, s74, 4194303\n\t"
    "v_mfma_f32_32x32x16_bf16 v[16:31], a[176:179], v[204:207], v[16:31]\n\t"
    "s_add_u32 m0, s75, 147456\n\t"
    "s_add_u32 s76, s75, 0\n\t"
    "buffer_load_dwordx4 v106, s[44:47], s76 offen nt lds\n\t"
    "v_mfma_f32_32x32x16_bf16 v[32:47], a[180:183], v[204:207], v[32:47]\n\t"
    "buffer_load_dwordx4 v107, s[44:47], s76 offen offset:1024 nt lds\n\t"
    "v_mfma_f32_32x32x16_bf16 v[48:63], a[184:187], v[204:207], v[48:63]\n\t"
    "buffer_load_dwordx4 v108, s[44:47], s76 offen offset:2048 nt lds\n\t"
    "v_mfma_f32_32x32x16_bf16 v[64:79], a[188:191], v[204:207], v[64:79]\n\t"
    "buffer_load_dwordx4 v109, s[44:47], s76 offen offset:3072 nt lds\n\t"
    "s_waitcnt vmcnt(24) lgkmcnt(0)\n\t"
    "s_barrier\n\t"
    "v_mfma_f32_32x32x16_bf16 v[16:31], a[128:131], v[208:211], v[16:31]\n\t"
    "ds_read_b64_tr_b16 a[160:161], v124 offset:32768\n\t"
    "ds_read_b64_tr_b16 a[162:163], v124 offset:33792\n\t"
    "ds_read_b64_tr_b16 a[164:165], v125 offset:32768\n\t"
    "ds_read_b64_tr_b16 a[166:167], v125 offset:33792\n\t"
    "v_mfma_f32_32x32x16_bf16 v[32:47], a[132:135], v[208:211], v[32:47]\n\t"
    "ds_read_b64_tr_b16 a[168:169], v126 offset:32768\n\t"
    "ds_read_b64_tr_b16 a[170:171], v126 offset:33792\n\t"
    "ds_read_b64_tr_b16 a[172:173], v127 offset:32768\n\t"
    "ds_read_b64_tr_b16 a[174:175], v127 offset:33792\n\t"
    "v_mfma_f32_32x32x16_bf16 v[48:63], a[136:139], v[208:211], v[48:63]\n\t"
    "ds_read_b64_tr_b16 a[176:177], v124 offset:36864\n\t"
    "ds_read_b64_tr_b16 a[178:179], v124 offset:37888\n\t"
    "ds_read_b64_tr_b16 a[180:181], v125 offset:36864\n\t"
    "ds_read_b64_tr_b16 a[182:183], v125 offset:37888\n\t"
    "v_mfma_f32_32x32x16_bf16 v[64:79], a[140:143], v[208:211], v[64:79]\n\t"
    "ds_read_b64_tr_b16 a[184:185], v126 offset:36864\n\t"
    "ds_read_b64_tr_b16 a[186:187], v126 offset:37888\n\t"
    "ds_read_b64_tr_b16 a[188:189], v127 offset:36864\n\t"
    "ds_read_b64_tr_b16 a[190:191], v127 offset:37888\n\t"
    "v_mfma_f32_32x32x16_bf16 v[16:31], a[144:147], v[212:215], v[16:31]\n\t"
    "v_mfma_f32_32x32x16_bf16 v[32:47], a[148:151], v[212:215], v[32:47]\n\t"
    "v_mfma_f32_32x32x16_bf16 v[48:63], a[152:155], v[212:215], v[48:63]\n\t"
    "v_mfma_f32_32x32x16_bf16 v[64:79], a[156:159], v[212:215], v[64:79]\n\t"
    "s_waitcnt vmcnt(12) lgkmcnt(0)\n\t"
    "s_barrier\n\t"
    "s_lshl_b32 s26, s83, 13\n\t"
    "s_add_u32 s26, s26, 98304\n\t"
    "v_add_u32 v9, s26, v82\n\t"
    "ds_read_b128 v[224:227], v9\n\t"
    "v_add_u32 v10, s26, v83\n\t"
    "ds_read_b128 v[228:231], v10\n\t"
    "v_add_u32 v11, s26, v84\n\t"
    "ds_read_b128 v[232:235], v11\n\t"
    "v_add_u32 v9, s26, v85\n\t"
    "ds_read_b128 v[236:239], v9\n\t"
    "v_add_u32 v10, s26, v86\n\t"
    "ds_read_b128 v[240:243], v10\n\t"
    "v_add_u32 v11, s26, v87\n\t"
    "ds_read_b128 v[244:247], v11\n\t"
    "v_add_u32 v9, s26, v88\n\t"
    "ds_read_b128 v[248:251], v9\n\t"
    "v_add_u32 v10, s26, v89\n\t"
    "ds_read_b128 v[252:255], v10\n\t"
    "v_mfma_f32_32x32x16_bf16 v[16:31], a[160:163], v[216:219], v[16:31]\n\t"
    "ds_read_b64_tr_b16 a[128:129], v116\n\t"
    "ds_read_b64_tr_b16 a[130:131], v116 offset:1024\n\t"
    "ds_read_b64_tr_b16 a[132:133], v117\n\t"
    "ds_read_b64_tr_b16 a[134:135], v117 offset:1024\n\t"
    "s_add_u32 m0, s75, 0\n\t"
    "s_add_u32 s76, s75, 16384\n\t"
    "buffer_load_dwordx4 v106, s[44:47], s76 offen nt lds\n\t"
    "v_mfma_f32_32x32x16_bf16 v[32:47], a[164:167], v[216:219], v[32:47]\n\t"
    "ds_read_b64_tr_b16 a[136:137], v118\n\t"
    "ds_read_b64_tr_b16 a[138:139], v118 offset:1024\n\t"
    "ds_read_b64_tr_b16 a[140:141], v119\n\t"
    "ds_read_b64_tr_b16 a[142:143], v119 offset:1024\n\t"
    "buffer_load_dwordx4 v107, s[44:47], s76 offen offset:1024 nt lds\n\t"
    "v_mfma_f32_32x32x16_bf16 v[48:63], a[168:171], v[216:219], v[48:63]\n\t"
    "ds_read_b64_tr_b16 a[144:145], v116 offset:4096\n\t"
    "ds_read_b64_tr_b16 a[146:147], v116 offset:5120\n\t"
    "ds_read_b64_tr_b16 a[148:149], v117 offset:4096\n\t"
    "ds_read_b64_tr_b16 a[150:151], v117 offset:5120\n\t"
    "buffer_load_dwordx4 v108, s[44:47], s76 offen offset:2048 nt lds\n\t"
    "v_mfma_f32_32x32x16_bf16 v[64:79], a[172:175], v[216:219], v[64:79]\n\t"
    "ds_read_b64_tr_b16 a[152:153], v118 offset:4096\n\t"
    "ds_read_b64_tr_b16 a[154:155], v118 offset:5120\n\t"
    "ds_read_b64_tr_b16 a[156:157], v119 offset:4096\n\t"
    "ds_read_b64_tr_b16 a[158:159], v119 offset:5120\n\t"
    "buffer_load_dwordx4 v109, s[44:47], s76 offen offset:3072 nt lds\n\t"
    "v_mfma_f32_32x32x16_bf16 v[16:31], a[176:179], v[220:223], v[16:31]\n\t"
    "v_mfma_f32_32x32x16_bf16 v[32:47], a[180:183], v[220:223], v[32:47]\n\t"
    "v_mfma_f32_32x32x16_bf16 v[48:63], a[184:187], v[220:223], v[48:63]\n\t"
    "v_mfma_f32_32x32x16_bf16 v[64:79], a[188:191], v[220:223], v[64:79]\n\t"
    "s_waitcnt lgkmcnt(0)\n\t"
    "s_barrier\n\t"
    "v_mfma_f32_32x32x16_bf16 v[16:31], a[128:131], v[224:227], v[16:31]\n\t"
    "ds_read_b64_tr_b16 a[160:161], v116 offset:16384\n\t"
    "ds_read_b64_tr_b16 a[162:163], v116 offset:17408\n\t"
    "ds_read_b64_tr_b16 a[164:165], v117 offset:16384\n\t"
    "ds_read_b64_tr_b16 a[166:167], v117 offset:17408\n\t"
    "s_add_u32 m0, s75, 16384\n\t"
    "s_add_u32 s76, s75, 0\n\t"
    "buffer_load_dwordx4 v110, s[48:51], s76 offen nt lds\n\t"
    "v_mfma_f32_32x32x16_bf16 v[32:47], a[132:135], v[224:227], v[32:47]\n\t"
    "ds_read_b64_tr_b16 a[168:169], v118 offset:16384\n\t"
    "ds_read_b64_tr_b16 a[170:171], v118 offset:17408\n\t"
    "ds_read_b64_tr_b16 a[172:173], v119 offset:16384\n\t"
    "ds_read_b64_tr_b16 a[174:175], v119 offset:17408\n\t"
    "buffer_load_dwordx4 v110, s[48:51], s76 offen offset:1024 nt lds\n\t"
    "v_mfma_f32_32x32x16_bf16 v[48:63], a[136:139], v[224:227], v[48:63]\n\t"
    "ds_read_b64_tr_b16 a[176:177], v116 offset:20480\n\t"
    "ds_read_b64_tr_b16 a[178:179], v116 offset:21504\n\t"
    "ds_read_b64_tr_b16 a[180:181], v117 offset:20480\n\t"
    "ds_read_b64_tr_b16 a[182:183], v117 offset:21504\n\t"
    "buffer_load_dwordx4 v110, s[48:51], s76 offen offset:2048 nt lds\n\t"
    "v_mfma_f32_32x32x16_bf16 v[64:79], a[140:143], v[224:227], v[64:79]\n\t"
    "ds_read_b64_tr_b16 a[184:185], v118 offset:20480\n\t"
    "ds_read_b64_tr_b16 a[186:187], v118 offset:21504\n\t"
    "ds_read_b64_tr_b16 a[188:189], v119 offset:20480\n\t"
    "ds_read_b64_tr_b16 a[190:191], v119 offset:21504\n\t"
    "buffer_load_dwordx4 v110, s[48:51], s76 offen offset:3072 nt lds\n\t"
    "v_mfma_f32_32x32x16_bf16 v[16:31], a[144:147], v[228:231], v[16:31]\n\t"
    "v_mfma_f32_32x32x16_bf16 v[32:47], a[148:151], v[228:231], v[32:47]\n\t"
    "v_mfma_f32_32x32x16_bf16 v[48:63], a[152:155], v[228:231], v[48:63]\n\t"
    "v_mfma_f32_32x32x16_bf16 v[64:79], a[156:159], v[228:231], v[64:79]\n\t"
    "s_waitcnt vmcnt(16) lgkmcnt(0)\n\t"
    "s_barrier\n\t"
    "v_mfma_f32_32x32x16_bf16 v[16:31], a[160:163], v[232:235], v[16:31]\n\t"
    "ds_read_b64_tr_b16 a[128:129], v116 offset:32768\n\t"
    "ds_read_b64_tr_b16 a[130:131], v116 offset:33792\n\t"
    "ds_read_b64_tr_b16 a[132:133], v117 offset:32768\n\t"
    "ds_read_b64_tr_b16 a[134:135], v117 offset:33792\n\t"
    "v_mfma_f32_32x32x16_bf16 v[32:47], a[164:167], v[232:235], v[32:47]\n\t"
    "ds_read_b64_tr_b16 a[136:137], v118 offset:32768\n\t"
    "ds_read_b64_tr_b16 a[138:139], v118 offset:33792\n\t"
    "ds_read_b64_tr_b16 a[140:141], v119 offset:32768\n\t"
    "ds_read_b64_tr_b16 a[142:143], v119 offset:33792\n\t"
    "v_mfma_f32_32x32x16_bf16 v[48:63], a[168:171], v[232:235], v[48:63]\n\t"
    "ds_read_b64_tr_b16 a[144:145], v116 offset:36864\n\t"
    "ds_read_b64_tr_b16 a[146:147], v116 offset:37888\n\t"
    "ds_read_b64_tr_b16 a[148:149], v117 offset:36864\n\t"
    "ds_read_b64_tr_b16 a[150:151], v117 offset:37888\n\t"
    "v_mfma_f32_32x32x16_bf16 v[64:79], a[172:175], v[232:235], v[64:79]\n\t"
    "ds_read_b64_tr_b16 a[152:153], v118 offset:36864\n\t"
    "ds_read_b64_tr_b16 a[154:155], v118 offset:37888\n\t"
    "ds_read_b64_tr_b16 a[156:157], v119 offset:36864\n\t"
    "ds_read_b64_tr_b16 a[158:159], v119 offset:37888\n\t"
    "v_mfma_f32_32x32x16_bf16 v[16:31], a[176:179], v[236:239], v[16:31]\n\t"
    "v_mfma_f32_32x32x16_bf16 v[32:47], a[180:183], v[236:239], v[32:47]\n\t"
    "v_mfma_f32_32x32x16_bf16 v[48:63], a[184:187], v[236:239], v[48:63]\n\t"
    "v_mfma_f32_32x32x16_bf16 v[64:79], a[188:191], v[236:239], v[64:79]\n\t"
    "s_waitcnt vmcnt(8) lgkmcnt(0)\n\t"
    "s_barrier\n\t"
    "v_add_u32 v2, 147456, v82\n\t"
    "v_add_u32 v3, 147456, v83\n\t"
    "v_add_u32 v4, 147456, v84\n\t"
    "v_add_u32 v5, 147456, v85\n\t"
    "v_add_u32 v6, 147456, v86\n\t"
    "v_add_u32 v7, 147456, v87\n\t"
    "v_add_u32 v8, 147456, v88\n\t"
    "v_add_u32 v9, 147456, v89\n\t"
    "v_mfma_f32_32x32x16_bf16 v[16:31], a[128:131], v[240:243], v[16:31]\n\t"
    "ds_read_b64_tr_b16 a[160:161], v116 offset:49152\n\t"
    "ds_read_b64_tr_b16 a[162:163], v116 offset:50176\n\t"
    "ds_read_b64_tr_b16 a[164:165], v117 offset:49152\n\t"
    "ds_read_b64_tr_b16 a[166:167], v117 offset:50176\n\t"
    "v_mfma_f32_32x32x16_bf16 v[32:47], a[132:135], v[240:243], v[32:47]\n\t"
    "ds_read_b64_tr_b16 a[168:169], v118 offset:49152\n\t"
    "ds_read_b64_tr_b16 a[170:171], v118 offset:50176\n\t"
    "ds_read_b64_tr_b16 a[172:173], v119 offset:49152\n\t"
    "ds_read_b64_tr_b16 a[174:175], v119 offset:50176\n\t"
    "v_mfma_f32_32x32x16_bf16 v[48:63], a[136:139], v[240:243], v[48:63]\n\t"
    "ds_read_b64_tr_b16 a[176:177], v116 offset:53248\n\t"
    "ds_read_b64_tr_b16 a[178:179], v116 offset:54272\n\t"
    "ds_read_b64_tr_b16 a[180:181], v117 offset:53248\n\t"
    "ds_read_b64_tr_b16 a[182:183], v117 offset:54272\n\t"
    "v_mfma_f32_32x32x16_bf16 v[64:79], a[140:143], v[240:243], v[64:79]\n\t"
    "ds_read_b64_tr_b16 a[184:185], v118 offset:53248\n\t"
    "ds_read_b64_tr_b16 a[186:187], v118 offset:54272\n\t"
    "ds_read_b64_tr_b16 a[188:189], v119 offset:53248\n\t"
    "ds_read_b64_tr_b16 a[190:191], v119 offset:54272\n\t"
    "v_mfma_f32_32x32x16_bf16 v[16:31], a[144:147], v[244:247], v[16:31]\n\t"
    "ds_read_b128 a[0:3], v2\n\t"
    "ds_read_b128 a[4:7], v3\n\t"
    "ds_read_b128 a[8:11], v4\n\t"
    "ds_read_b128 a[12:15], v5\n\t"
    "v_mfma_f32_32x32x16_bf16 v[32:47], a[148:151], v[244:247], v[32:47]\n\t"
    "ds_read_b128 a[16:19], v6\n\t"
    "ds_read_b128 a[20:23], v7\n\t"
    "ds_read_b128 a[24:27], v8\n\t"
    "ds_read_b128 a[28:31], v9\n\t"
    "v_mfma_f32_32x32x16_bf16 v[48:63], a[152:155], v[244:247], v[48:63]\n\t"
    "ds_read_b128 a[32:35], v2 offset:8192\n\t"
    "ds_read_b128 a[36:39], v3 offset:8192\n\t"
    "ds_read_b128 a[40:43], v4 offset:8192\n\t"
    "ds_read_b128 a[44:47], v5 offset:8192\n\t"
    "v_mfma_f32_32x32x16_bf16 v[64:79], a[156:159], v[244:247], v[64:79]\n\t"
    "ds_read_b128 a[48:51], v6 offset:8192\n\t"
    "ds_read_b128 a[52:55], v7 offset:8192\n\t"
    "ds_read_b128 a[56:59], v8 offset:8192\n\t"
    "ds_read_b128 a[60:63], v9 offset:8192\n\t"
    "s_waitcnt lgkmcnt(0)\n\t"
    "s_barrier\n\t"
    "v_mfma_f32_32x32x16_bf16 v[16:31], a[160:163], v[248:251], v[16:31]\n\t"
    "v_mfma_f32_32x32x16_bf16 v[32:47], a[164:167], v[248:251], v[32:47]\n\t"
    "v_mfma_f32_32x32x16_bf16 v[48:63], a[168:171], v[248:251], v[48:63]\n\t"
    "v_mfma_f32_32x32x16_bf16 v[64:79], a[172:175], v[248:251], v[64:79]\n\t"
    "v_mfma_f32_32x32x16_bf16 v[16:31], a[176:179], v[252:255], v[16:31]\n\t"
    "v_mfma_f32_32x32x16_bf16 v[32:47], a[180:183], v[252:255], v[32:47]\n\t"
    "v_mfma_f32_32x32x16_bf16 v[48:63], a[184:187], v[252:255], v[48:63]\n\t"
    "v_mfma_f32_32x32x16_bf16 v[64:79], a[188:191], v[252:255], v[64:79]\n\t"
    "s_barrier\n\t"
    "v_readfirstlane_b32 s94, v114\n\t"
    "v_readfirstlane_b32 s95, v115\n\t"
    "s_add_u32 s94, s94, 1\n\t"
    "s_addc_u32 s95, s95, 0\n\t"
    "s_mov_b32 s30, 0x3e0293ee\n\t"
    "s_mov_b32 s31, 0x3e0293ee\n\t"
    "s_nop 1\n\t"
    "v_pk_mul_f32 v[16:17], v[16:17], s[30:31]\n\t"
    "s_nop 0\n\t"
    "v_pk_mul_f32 v[18:19], v[18:19], s[30:31]\n\t"
    "v_pk_mul_f32 v[20:21], v[20:21], s[30:31]\n\t"
    "v_pk_mul_f32 v[22:23], v[22:23], s[30:31]\n\t"
    "v_pk_mul_f32 v[24:25], v[24:25], s[30:31]\n\t"
    "v_pk_mul_f32 v[26:27], v[26:27], s[30:31]\n\t"
    "v_pk_mul_f32 v[28:29], v[28:29], s[30:31]\n\t"
    "v_pk_mul_f32 v[30:31], v[30:31], s[30:31]\n\t"
    "s_add_u32 m0, s75, 32768\n\t"
    "s_add_u32 s76, s75, 32768\n\t"
    "buffer_load_dwordx4 v106, s[44:47], s76 offen nt lds\n\t"
    "v_cvt_pk_bf16_f32 v128, v16, v17\n\t"
    "v_cvt_pk_bf16_f32 v129, v18, v19\n\t"
    "v_cvt_pk_bf16_f32 v130, v20, v21\n\t"
    "v_cvt_pk_bf16_f32 v131, v22, v23\n\t"
    "v_cvt_pk_bf16_f32 v132, v24, v25\n\t"
    "v_cvt_pk_bf16_f32 v133, v26, v27\n\t"
    "v_cvt_pk_bf16_f32 v134, v28, v29\n\t"
    "v_cvt_pk_bf16_f32 v135, v30, v31\n\t"
    "buffer_load_dwordx4 v107, s[44:47], s76 offen offset:1024 nt lds\n\t"
    "v_mov_b32 v80, 0\n\t"
    "s_mov_b32 s97, 0\n\t"
    "s_lshl_b32 s74, s84, 16\n\t"
    "s_waitcnt vmcnt(2)\n\t"
    "s_barrier\n\t"
    "v_mfma_f32_32x32x16_bf16 v[16:31], a[0:3], v[128:131], 0\n\t"
    "ds_read_b64_tr_b16 a[64:65], v98 offset:16384\n\t"
    "v_mfma_f32_32x32x16_bf16 v[16:31], a[4:7], v[132:135], v[16:31]\n\t"
    "ds_read_b128 a[0:3], v82\n\t"
    "ds_read_b64_tr_b16 a[66:67], v98 offset:18432\n\t"
    "v_pk_mul_f32 v[32:33], v[32:33], s[30:31]\n\t"
    "v_pk_mul_f32 v[34:35], v[34:35], s[30:31]\n\t"
    "v_pk_mul_f32 v[36:37], v[36:37], s[30:31]\n\t"
    "v_pk_mul_f32 v[38:39], v[38:39], s[30:31]\n\t"
    "v_pk_mul_f32 v[40:41], v[40:41], s[30:31]\n\t"
    "v_pk_mul_f32 v[42:43], v[42:43], s[30:31]\n\t"
    "v_pk_mul_f32 v[44:45], v[44:45], s[30:31]\n\t"
    "v_pk_mul_f32 v[46:47], v[46:47], s[30:31]\n\t"
    "buffer_load_dwordx4 v108, s[44:47], s76 offen offset:2048 nt lds\n\t"
    "v_cvt_pk_bf16_f32 v136, v32, v33\n\t"
    "v_cvt_pk_bf16_f32 v137, v34, v35\n\t"
    "v_cvt_pk_bf16_f32 v138, v36, v37\n\t"
    "v_cvt_pk_bf16_f32 v139, v38, v39\n\t"
    "v_cvt_pk_bf16_f32 v140, v40, v41\n\t"
    "v_cvt_pk_bf16_f32 v141, v42, v43\n\t"
    "v_cvt_pk_bf16_f32 v142, v44, v45\n\t"
    "v_cvt_pk_bf16_f32 v143, v46, v47\n\t"
    "buffer_load_dwordx4 v109, s[44:47], s76 offen offset:3072 nt lds\n\t"
    "v_mfma_f32_32x32x16_bf16 v[16:31], a[8:11], v[136:139], v[16:31]\n\t"
    "ds_read_b64_tr_b16 a[68:69], v99 offset:16384\n\t"
    "v_mfma_f32_32x32x16_bf16 v[16:31], a[12:15], v[140:143], v[16:31]\n\t"
    "ds_read_b128 a[4:7], v83\n\t"
    "ds_read_b64_tr_b16 a[70:71], v99 offset:18432\n\t"
    "v_pk_mul_f32 v[48:49], v[48:49], s[30:31]\n\t"
    "v_pk_mul_f32 v[50:51], v[50:51], s[30:31]\n\t"
    "v_pk_mul_f32 v[52:53], v[52:53], s[30:31]\n\t"
    "v_pk_mul_f32 v[54:55], v[54:55], s[30:31]\n\t"
    "v_pk_mul_f32 v[56:57], v[56:57], s[30:31]\n\t"
    "v_pk_mul_f32 v[58:59], v[58:59], s[30:31]\n\t"
    "v_pk_mul_f32 v[60:61], v[60:61], s[30:31]\n\t"
    "v_pk_mul_f32 v[62:63], v[62:63], s[30:31]\n\t"
    "s_add_u32 m0, s75, 49152\n\t"
    "s_add_u32 s76, s75, 16384\n\t"
    "buffer_load_dwordx4 v110, s[48:51], s76 offen nt lds\n\t"
    "v_cvt_pk_bf16_f32 v144, v48, v49\n\t"
    "v_cvt_pk_bf16_f32 v145, v50, v51\n\t"
    "v_cvt_pk_bf16_f32 v146, v52, v53\n\t"
    "v_cvt_pk_bf16_f32 v147, v54, v55\n\t"
    "v_cvt_pk_bf16_f32 v148, v56, v57\n\t"
    "v_cvt_pk_bf16_f32 v149, v58, v59\n\t"
    "v_cvt_pk_bf16_f32 v150, v60, v61\n\t"
    "v_cvt_pk_bf16_f32 v151, v62, v63\n\t"
    "buffer_load_dwordx4 v110, s[48:51], s76 offen offset:1024 nt lds\n\t"
    "v_mfma_f32_32x32x16_bf16 v[16:31], a[16:19], v[144:147], v[16:31]\n\t"
    "ds_read_b64_tr_b16 a[72:73], v100 offset:16384\n\t"
    "v_mfma_f32_32x32x16_bf16 v[16:31], a[20:23], v[148:151], v[16:31]\n\t"
    "ds_read_b128 a[8:11], v84\n\t"
    "ds_read_b64_tr_b16 a[74:75], v100 offset:18432\n\t"
    "v_pk_mul_f32 v[64:65], v[64:65], s[30:31]\n\t"
    "v_pk_mul_f32 v[66:67], v[66:67], s[30:31]\n\t"
    "v_pk_mul_f32 v[68:69], v[68:69], s[30:31]\n\t"
    "v_pk_mul_f32 v[70:71], v[70:71], s[30:31]\n\t"
    "v_pk_mul_f32 v[72:73], v[72:73], s[30:31]\n\t"
    "v_pk_mul_f32 v[74:75], v[74:75], s[30:31]\n\t"
    "v_pk_mul_f32 v[76:77], v[76:77], s[30:31]\n\t"
    "v_pk_mul_f32 v[78:79], v[78:79], s[30:31]\n\t"
    "buffer_load_dwordx4 v110, s[48:51], s76 offen offset:2048 nt lds\n\t"
    "v_cvt_pk_bf16_f32 v152, v64, v65\n\t"
    "v_cvt_pk_bf16_f32 v153, v66, v67\n\t"
    "v_cvt_pk_bf16_f32 v154, v68, v69\n\t"
    "v_cvt_pk_bf16_f32 v155, v70, v71\n\t"
    "v_cvt_pk_bf16_f32 v156, v72, v73\n\t"
    "v_cvt_pk_bf16_f32 v157, v74, v75\n\t"
    "v_cvt_pk_bf16_f32 v158, v76, v77\n\t"
    "v_cvt_pk_bf16_f32 v159, v78, v79\n\t"
    "buffer_load_dwordx4 v110, s[48:51], s76 offen offset:3072 nt lds\n\t"
    "v_mfma_f32_32x32x16_bf16 v[16:31], a[24:27], v[152:155], v[16:31]\n\t"
    "ds_read_b64_tr_b16 a[76:77], v101 offset:16384\n\t"
    "v_mfma_f32_32x32x16_bf16 v[16:31], a[28:31], v[156:159], v[16:31]\n\t"
    "ds_read_b128 a[12:15], v85\n\t"
    "ds_read_b64_tr_b16 a[78:79], v101 offset:18432\n\t"
    "v_and_b32 v2, 31, v1\n\t"
    "v_lshrrev_b32 v3, 5, v1\n\t"
    "v_and_b32 v4, 15, v1\n\t"
    "v_lshrrev_b32 v5, 4, v1\n\t"
    "v_bfe_u32 v6, v1, 2, 2\n\t"
    "v_and_b32 v7, 3, v1\n\t"
    "v_bfe_u32 v8, v1, 4, 1\n\t"
    "v_lshrrev_b32 v9, 3, v1\n\t"
    "v_and_b32 v10, 7, v1\n\t"
    "v_bfe_u32 v11, v1, 4, 1\n\t"
    "v_lshlrev_b32 v11, 2, v11\n\t"
    "v_xor_b32 v10, v10, v11\n\t"
    "v_lshlrev_b32 v10, 4, v10\n\t"
    "v_lshl_add_u32 v70, v9, 10, v10\n\t"
    "v_add_u32 v71, 7168, v70\n\t"
    "v_lshl_add_u32 v9, v3, 2, v6\n\t"
    "v_lshlrev_b32 v9, 7, v9\n\t"
    "v_lshrrev_b32 v10, 1, v7\n\t"
    "v_lshl_add_u32 v9, v10, 3, v9\n\t"
    "v_add_u32 v9, s80, v9\n\t"
    "v_and_b32 v10, 1, v7\n\t"
    "v_lshl_add_u32 v10, v8, 1, v10\n\t"
    "v_lshrrev_b32 v11, 1, v6\n\t"
    "v_lshlrev_b32 v11, 2, v11\n\t"
    "v_add_u32 v12, 0, v10\n\t"
    "v_xor_b32 v12, v12, v11\n\t"
    "v_lshl_add_u32 v64, v12, 4, v9\n\t"
    "v_add_u32 v66, 65536, v64\n\t"
    "v_add_u32 v68, 131072, v64\n\t"
    "v_add_u32 v12, 4, v10\n\t"
    "v_xor_b32 v12, v12, v11\n\t"
    "v_lshl_add_u32 v65, v12, 4, v9\n\t"
    "v_add_u32 v67, 65536, v65\n\t"
    "v_add_u32 v69, 131072, v65\n\t"
    "s_lshl_b32 s26, s82, 3\n\t"
    "s_add_u32 s26, s26, 16\n\t"
    "s_lshl_b32 s27, s84, 2\n\t"
    "s_sub_u32 s26, s26, s27\n\t"
    "s_and_b32 s26, s26, 15\n\t"
    "s_lshl_b32 s26, s26, 1\n\t"
    "s_add_u32 s26, s26, s83\n\t"
    "s_lshl_b32 s26, s26, 10\n\t"
    "s_add_u32 s26, s26, 65536\n\t"
    "v_add_u32 v72, s26, v120\n\t"
    "s_lshl_b32 s26, s82, 3\n\t"
    "s_add_u32 s26, s26, 20\n\t"
    "s_lshl_b32 s27, s84, 2\n\t"
    "s_sub_u32 s26, s26, s27\n\t"
    "s_and_b32 s26, s26, 15\n\t"
    "s_lshl_b32 s26, s26, 1\n\t"
    "s_add_u32 s26, s26, s83\n\t"
    "s_lshl_b32 s26, s26, 10\n\t"
    "s_add_u32 s26, s26, 65536\n\t"
    "v_add_u32 v73, s26, v120\n\t"
    "v_add_u32 v122, 65536, v120\n\t"
    "s_add_u32 s76, s75, 49152\n\t"
    "s_add_u32 m0, s75, 65536\n\t"
    "ds_read_b64_tr_b16 a[80:81], v98 offset:20480\n\t"
    "buffer_load_dwordx4 v106, s[44:47], s76 offen nt lds\n\t"
    "ds_read_b128 a[16:19], v86\n\t"
    "ds_read_b64_tr_b16 a[82:83], v98 offset:22528\n\t"
    "ds_read_b64_tr_b16 a[84:85], v99 offset:20480\n\t"
    "buffer_load_dwordx4 v107, s[44:47], s76 offen offset:1024 nt lds\n\t"
    "ds_read_b128 a[20:23], v87\n\t"
    "ds_read_b64_tr_b16 a[86:87], v99 offset:22528\n\t"
    "v_exp_f32 v16, v16\n\t"
    "ds_read_b64_tr_b16 a[88:89], v100 offset:20480\n\t"
    "v_add_f32 v80, v80, v16\n\t"
    "v_exp_f32 v17, v17\n\t"
    "buffer_load_dwordx4 v108, s[44:47], s76 offen offset:2048 nt lds\n\t"
    "ds_read_b128 a[24:27], v88\n\t"
    "ds_read_b64_tr_b16 a[90:91], v100 offset:22528\n\t"
    "v_cvt_pk_bf16_f32 v48, v16, v17\n\t"
    "v_add_f32 v80, v80, v17\n\t"
    "v_exp_f32 v18, v18\n\t"
    "ds_read_b64_tr_b16 a[92:93], v101 offset:20480\n\t"
    "v_add_f32 v80, v80, v18\n\t"
    "v_exp_f32 v19, v19\n\t"
    "buffer_load_dwordx4 v109, s[44:47], s76 offen offset:3072 nt lds\n\t"
    "ds_read_b128 a[28:31], v89\n\t"
    "ds_read_b64_tr_b16 a[94:95], v101 offset:22528\n\t"
    "v_cvt_pk_bf16_f32 v49, v18, v19\n\t"
    "v_add_f32 v80, v80, v19\n\t"
    "v_exp_f32 v20, v20\n\t"
    "v_mfma_f32_32x32x16_bf16 v[32:47], a[32:35], v[128:131], 0\n\t"
    "s_add_u32 s76, s75, 32768\n\t"
    "s_add_u32 m0, s75, 81920\n\t"
    "ds_read_b64_tr_b16 a[96:97], v98 offset:24576\n\t"
    "v_add_f32 v80, v80, v20\n\t"
    "v_exp_f32 v21, v21\n\t"
    "v_mfma_f32_32x32x16_bf16 v[32:47], a[36:39], v[132:135], v[32:47]\n\t"
    "ds_read_b64_tr_b16 a[98:99], v98 offset:26624\n\t"
    "ds_read_b128 a[32:35], v82 offset:8192\n\t"
    "v_cvt_pk_bf16_f32 v50, v20, v21\n\t"
    "v_add_f32 v80, v80, v21\n\t"
    "v_exp_f32 v22, v22\n\t"
    "v_mfma_f32_32x32x16_bf16 v[32:47], a[40:43], v[136:139], v[32:47]\n\t"
    "buffer_load_dwordx4 v110, s[48:51], s76 offen nt lds\n\t"
    "ds_read_b64_tr_b16 a[100:101], v99 offset:24576\n\t"
    "v_add_f32 v80, v80, v22\n\t"
    "v_exp_f32 v23, v23\n\t"
    "v_mfma_f32_32x32x16_bf16 v[32:47], a[44:47], v[140:143], v[32:47]\n\t"
    "ds_read_b64_tr_b16 a[102:103], v99 offset:26624\n\t"
    "ds_read_b128 a[36:39], v83 offset:8192\n\t"
    "v_cvt_pk_bf16_f32 v51, v22, v23\n\t"
    "v_add_f32 v80, v80, v23\n\t"
    "v_exp_f32 v24, v24\n\t"
    "v_mfma_f32_32x32x16_bf16 v[32:47], a[48:51], v[144:147], v[32:47]\n\t"
    "ds_read_b64_tr_b16 a[104:105], v100 offset:24576\n\t"
    "v_add_f32 v80, v80, v24\n\t"
    "v_exp_f32 v25, v25\n\t"
    "v_mfma_f32_32x32x16_bf16 v[32:47], a[52:55], v[148:151], v[32:47]\n\t"
    "ds_read_b64_tr_b16 a[106:107], v100 offset:26624\n\t"
    "ds_read_b128 a[40:43], v84 offset:8192\n\t"
    "v_cvt_pk_bf16_f32 v52, v24, v25\n\t"
    "v_add_f32 v80, v80, v25\n\t"
    "v_exp_f32 v26, v26\n\t"
    "v_mfma_f32_32x32x16_bf16 v[32:47], a[56:59], v[152:155], v[32:47]\n\t"
    "buffer_load_dwordx4 v110, s[48:51], s76 offen offset:1024 nt lds\n\t"
    "ds_read_b64_tr_b16 a[108:109], v101 offset:24576\n\t"
    "v_add_f32 v80, v80, v26\n\t"
    "v_exp_f32 v27, v27\n\t"
    "v_mfma_f32_32x32x16_bf16 v[32:47], a[60:63], v[156:159], v[32:47]\n\t"
    "ds_read_b64_tr_b16 a[110:111], v101 offset:26624\n\t"
    "ds_read_b128 a[44:47], v85 offset:8192\n\t"
    "v_cvt_pk_bf16_f32 v53, v26, v27\n\t"
    "v_add_f32 v80, v80, v27\n\t"
    "v_exp_f32 v28, v28\n\t"
    "s_waitcnt lgkmcnt(12)\n\t"
    "v_mfma_f32_32x32x16_bf16 v[160:175], a[64:67], v[48:51], 0\n\t"
    "ds_read_b64_tr_b16 a[112:113], v98 offset:28672\n\t"
    "v_add_f32 v80, v80, v28\n\t"
    "v_exp_f32 v29, v29\n\t"
    "v_mfma_f32_32x32x16_bf16 v[176:191], a[68:71], v[48:51], 0\n\t"
    "ds_read_b64_tr_b16 a[114:115], v98 offset:30720\n\t"
    "ds_read_b128 a[48:51], v86 offset:8192\n\t"
    "v_cvt_pk_bf16_f32 v54, v28, v29\n\t"
    "v_add_f32 v80, v80, v29\n\t"
    "v_exp_f32 v30, v30\n\t"
    "v_mfma_f32_32x32x16_bf16 v[192:207], a[72:75], v[48:51], 0\n\t"
    "buffer_load_dwordx4 v110, s[48:51], s76 offen offset:2048 nt lds\n\t"
    "ds_read_b64_tr_b16 a[116:117], v99 offset:28672\n\t"
    "v_add_f32 v80, v80, v30\n\t"
    "v_exp_f32 v31, v31\n\t"
    "v_mfma_f32_32x32x16_bf16 v[208:223], a[76:79], v[48:51], 0\n\t"
    "ds_read_b64_tr_b16 a[118:119], v99 offset:30720\n\t"
    "ds_read_b128 a[52:55], v87 offset:8192\n\t"
    "v_cvt_pk_bf16_f32 v55, v30, v31\n\t"
    "v_add_f32 v80, v80, v31\n\t"
    "v_exp_f32 v32, v32\n\t"
    "v_mfma_f32_32x32x16_bf16 v[160:175], a[80:83], v[52:55], v[160:175]\n\t"
    "ds_read_b64_tr_b16 a[120:121], v100 offset:28672\n\t"
    "v_add_f32 v80, v80, v32\n\t"
    "v_exp_f32 v33, v33\n\t"
    "v_mfma_f32_32x32x16_bf16 v[176:191], a[84:87], v[52:55], v[176:191]\n\t"
    "ds_read_b64_tr_b16 a[122:123], v100 offset:30720\n\t"
    "ds_read_b128 a[56:59], v88 offset:8192\n\t"
    "v_cvt_pk_bf16_f32 v56, v32, v33\n\t"
    "v_add_f32 v80, v80, v33\n\t"
    "v_exp_f32 v34, v34\n\t"
    "v_mfma_f32_32x32x16_bf16 v[192:207], a[88:91], v[52:55], v[192:207]\n\t"
    "buffer_load_dwordx4 v110, s[48:51], s76 offen offset:3072 nt lds\n\t"
    "ds_read_b64_tr_b16 a[124:125], v101 offset:28672\n\t"
    "v_add_f32 v80, v80, v34\n\t"
    "v_exp_f32 v35, v35\n\t"
    "v_mfma_f32_32x32x16_bf16 v[208:223], a[92:95], v[52:55], v[208:223]\n\t"
    "ds_read_b64_tr_b16 a[126:127], v101 offset:30720\n\t"
    "ds_read_b128 a[60:63], v89 offset:8192\n\t"
    "v_cvt_pk_bf16_f32 v57, v34, v35\n\t"
    "v_add_f32 v80, v80, v35\n\t"
    "v_exp_f32 v36, v36\n\t"
    "s_waitcnt vmcnt(8)\n\t"
    "s_barrier\n\t"
    "v_mfma_f32_32x32x16_bf16 v[16:31], a[0:3], v[128:131], 0\n\t"
    "s_add_u32 s76, s75, 65536\n\t"
    "s_add_u32 m0, s75, 98304\n\t"
    "ds_read_b64_tr_b16 a[64:65], v98 offset:49152\n\t"
    "v_add_f32 v80, v80, v36\n\t"
    "v_exp_f32 v37, v37\n\t"
    "v_mfma_f32_32x32x16_bf16 v[16:31], a[4:7], v[132:135], v[16:31]\n\t"
    "ds_read_b128 a[0:3], v82 offset:32768\n\t"
    "ds_read_b64_tr_b16 a[66:67], v98 offset:51200\n\t"
    "v_cvt_pk_bf16_f32 v58, v36, v37\n\t"
    "v_add_f32 v80, v80, v37\n\t"
    "v_exp_f32 v38, v38\n\t"
    "v_mfma_f32_32x32x16_bf16 v[16:31], a[8:11], v[136:139], v[16:31]\n\t"
    "buffer_load_dwordx4 v106, s[44:47], s76 offen nt lds\n\t"
    "ds_read_b64_tr_b16 a[68:69], v99 offset:49152\n\t"
    "v_add_f32 v80, v80, v38\n\t"
    "v_exp_f32 v39, v39\n\t"
    "v_mfma_f32_32x32x16_bf16 v[16:31], a[12:15], v[140:143], v[16:31]\n\t"
    "ds_read_b128 a[4:7], v83 offset:32768\n\t"
    "ds_read_b64_tr_b16 a[70:71], v99 offset:51200\n\t"
    "v_cvt_pk_bf16_f32 v59, v38, v39\n\t"
    "v_add_f32 v80, v80, v39\n\t"
    "v_exp_f32 v40, v40\n\t"
    "v_mfma_f32_32x32x16_bf16 v[16:31], a[16:19], v[144:147], v[16:31]\n\t"
    "ds_read_b64_tr_b16 a[72:73], v100 offset:49152\n\t"
    "v_add_f32 v80, v80, v40\n\t"
    "v_exp_f32 v41, v41\n\t"
    "v_mfma_f32_32x32x16_bf16 v[16:31], a[20:23], v[148:151], v[16:31]\n\t"
    "ds_read_b128 a[8:11], v84 offset:32768\n\t"
    "ds_read_b64_tr_b16 a[74:75], v100 offset:51200\n\t"
    "v_cvt_pk_bf16_f32 v60, v40, v41\n\t"
    "v_add_f32 v80, v80, v41\n\t"
    "v_exp_f32 v42, v42\n\t"
    "v_mfma_f32_32x32x16_bf16 v[16:31], a[24:27], v[152:155], v[16:31]\n\t"
    "buffer_load_dwordx4 v107, s[44:47], s76 offen offset:1024 nt lds\n\t"
    "ds_read_b64_tr_b16 a[76:77], v101 offset:49152\n\t"
    "v_add_f32 v80, v80, v42\n\t"
    "v_exp_f32 v43, v43\n\t"
    "v_mfma_f32_32x32x16_bf16 v[16:31], a[28:31], v[156:159], v[16:31]\n\t"
    "ds_read_b128 a[12:15], v85 offset:32768\n\t"
    "ds_read_b64_tr_b16 a[78:79], v101 offset:51200\n\t"
    "v_cvt_pk_bf16_f32 v61, v42, v43\n\t"
    "v_add_f32 v80, v80, v43\n\t"
    "v_exp_f32 v44, v44\n\t"
    "s_waitcnt lgkmcnt(13)\n\t"
    "v_mfma_f32_32x32x16_bf16 v[160:175], a[96:99], v[56:59], v[160:175]\n\t"
    "ds_read_b64_tr_b16 a[80:81], v98 offset:53248\n\t"
    "v_add_f32 v80, v80, v44\n\t"
    "v_exp_f32 v45, v45\n\t"
    "v_mfma_f32_32x32x16_bf16 v[176:191], a[100:103], v[56:59], v[176:191]\n\t"
    "ds_read_b128 a[16:19], v86 offset:32768\n\t"
    "ds_read_b64_tr_b16 a[82:83], v98 offset:55296\n\t"
    "v_cvt_pk_bf16_f32 v62, v44, v45\n\t"
    "v_add_f32 v80, v80, v45\n\t"
    "v_exp_f32 v46, v46\n\t"
    "v_mfma_f32_32x32x16_bf16 v[192:207], a[104:107], v[56:59], v[192:207]\n\t"
    "buffer_load_dwordx4 v108, s[44:47], s76 offen offset:2048 nt lds\n\t"
    "ds_read_b64_tr_b16 a[84:85], v99 offset:53248\n\t"
    "v_add_f32 v80, v80, v46\n\t"
    "v_exp_f32 v47, v47\n\t"
    "v_mfma_f32_32x32x16_bf16 v[208:223], a[108:111], v[56:59], v[208:223]\n\t"
    "ds_read_b128 a[20:23], v87 offset:32768\n\t"
    "ds_read_b64_tr_b16 a[86:87], v99 offset:55296\n\t"
    "v_cvt_pk_bf16_f32 v63, v46, v47\n\t"
    "v_add_f32 v80, v80, v47\n\t"
    "v_exp_f32 v16, v16\n\t"
    "v_mfma_f32_32x32x16_bf16 v[160:175], a[112:115], v[60:63], v[160:175]\n\t"
    "ds_read_b64_tr_b16 a[88:89], v100 offset:53248\n\t"
    "v_add_f32 v80, v80, v16\n\t"
    "v_exp_f32 v17, v17\n\t"
    "v_mfma_f32_32x32x16_bf16 v[176:191], a[116:119], v[60:63], v[176:191]\n\t"
    "ds_read_b128 a[24:27], v88 offset:32768\n\t"
    "ds_read_b64_tr_b16 a[90:91], v100 offset:55296\n\t"
    "v_cvt_pk_bf16_f32 v48, v16, v17\n\t"
    "v_add_f32 v80, v80, v17\n\t"
    "v_exp_f32 v18, v18\n\t"
    "v_mfma_f32_32x32x16_bf16 v[192:207], a[120:123], v[60:63], v[192:207]\n\t"
    "buffer_load_dwordx4 v109, s[44:47], s76 offen offset:3072 nt lds\n\t"
    "ds_read_b64_tr_b16 a[92:93], v101 offset:53248\n\t"
    "v_add_f32 v80, v80, v18\n\t"
    "v_exp_f32 v19, v19\n\t"
    "v_mfma_f32_32x32x16_bf16 v[208:223], a[124:127], v[60:63], v[208:223]\n\t"
    "ds_read_b128 a[28:31], v89 offset:32768\n\t"
    "ds_read_b64_tr_b16 a[94:95], v101 offset:55296\n\t"
    "v_cvt_pk_bf16_f32 v49, v18, v19\n\t"
    "v_add_f32 v80, v80, v19\n\t"
    "v_exp_f32 v20, v20\n\t"
    "s_waitcnt lgkmcnt(15)\n\t"
    "v_mfma_f32_32x32x16_bf16 v[32:47], a[32:35], v[128:131], 0\n\t"
    "s_add_u32 s76, s75, 49152\n\t"
    "s_add_u32 m0, s75, 114688\n\t"
    "ds_read_b64_tr_b16 a[96:97], v98 offset:57344\n\t"
    "v_add_f32 v80, v80, v20\n\t"
    "v_exp_f32 v21, v21\n\t"
    "v_mfma_f32_32x32x16_bf16 v[32:47], a[36:39], v[132:135], v[32:47]\n\t"
    "ds_read_b64_tr_b16 a[98:99], v98 offset:59392\n\t"
    "ds_read_b128 a[32:35], v82 offset:40960\n\t"
    "v_cvt_pk_bf16_f32 v50, v20, v21\n\t"
    "v_add_f32 v80, v80, v21\n\t"
    "v_exp_f32 v22, v22\n\t"
    "v_mfma_f32_32x32x16_bf16 v[32:47], a[40:43], v[136:139], v[32:47]\n\t"
    "buffer_load_dwordx4 v110, s[48:51], s76 offen nt lds\n\t"
    "ds_read_b64_tr_b16 a[100:101], v99 offset:57344\n\t"
    "v_add_f32 v80, v80, v22\n\t"
    "v_exp_f32 v23, v23\n\t"
    "v_mfma_f32_32x32x16_bf16 v[32:47], a[44:47], v[140:143], v[32:47]\n\t"
    "ds_read_b64_tr_b16 a[102:103], v99 offset:59392\n\t"
    "ds_read_b128 a[36:39], v83 offset:40960\n\t"
    "v_cvt_pk_bf16_f32 v51, v22, v23\n\t"
    "v_add_f32 v80, v80, v23\n\t"
    "v_exp_f32 v24, v24\n\t"
    "v_mfma_f32_32x32x16_bf16 v[32:47], a[48:51], v[144:147], v[32:47]\n\t"
    "ds_read_b64_tr_b16 a[104:105], v100 offset:57344\n\t"
    "v_add_f32 v80, v80, v24\n\t"
    "v_exp_f32 v25, v25\n\t"
    "v_mfma_f32_32x32x16_bf16 v[32:47], a[52:55], v[148:151], v[32:47]\n\t"
    "ds_read_b64_tr_b16 a[106:107], v100 offset:59392\n\t"
    "ds_read_b128 a[40:43], v84 offset:40960\n\t"
    "v_cvt_pk_bf16_f32 v52, v24, v25\n\t"
    "v_add_f32 v80, v80, v25\n\t"
    "v_exp_f32 v26, v26\n\t"
    "v_mfma_f32_32x32x16_bf16 v[32:47], a[56:59], v[152:155], v[32:47]\n\t"
    "buffer_load_dwordx4 v110, s[48:51], s76 offen offset:1024 nt lds\n\t"
    "ds_read_b64_tr_b16 a[108:109], v101 offset:57344\n\t"
    "v_add_f32 v80, v80, v26\n\t"
    "v_exp_f32 v27, v27\n\t"
    "v_mfma_f32_32x32x16_bf16 v[32:47], a[60:63], v[156:159], v[32:47]\n\t"
    "ds_read_b64_tr_b16 a[110:111], v101 offset:59392\n\t"
    "ds_read_b128 a[44:47], v85 offset:40960\n\t"
    "v_cvt_pk_bf16_f32 v53, v26, v27\n\t"
    "v_add_f32 v80, v80, v27\n\t"
    "v_exp_f32 v28, v28\n\t"
    "s_waitcnt lgkmcnt(12)\n\t"
    "v_mfma_f32_32x32x16_bf16 v[160:175], a[64:67], v[48:51], v[160:175]\n\t"
    "ds_read_b64_tr_b16 a[112:113], v98 offset:61440\n\t"
    "v_add_f32 v80, v80, v28\n\t"
    "v_exp_f32 v29, v29\n\t"
    "v_mfma_f32_32x32x16_bf16 v[176:191], a[68:71], v[48:51], v[176:191]\n\t"
    "ds_read_b64_tr_b16 a[114:115], v98 offset:63488\n\t"
    "ds_read_b128 a[48:51], v86 offset:40960\n\t"
    "v_cvt_pk_bf16_f32 v54, v28, v29\n\t"
    "v_add_f32 v80, v80, v29\n\t"
    "v_exp_f32 v30, v30\n\t"
    "v_mfma_f32_32x32x16_bf16 v[192:207], a[72:75], v[48:51], v[192:207]\n\t"
    "buffer_load_dwordx4 v110, s[48:51], s76 offen offset:2048 nt lds\n\t"
    "ds_read_b64_tr_b16 a[116:117], v99 offset:61440\n\t"
    "v_add_f32 v80, v80, v30\n\t"
    "v_exp_f32 v31, v31\n\t"
    "v_mfma_f32_32x32x16_bf16 v[208:223], a[76:79], v[48:51], v[208:223]\n\t"
    "ds_read_b64_tr_b16 a[118:119], v99 offset:63488\n\t"
    "ds_read_b128 a[52:55], v87 offset:40960\n\t"
    "v_cvt_pk_bf16_f32 v55, v30, v31\n\t"
    "v_add_f32 v80, v80, v31\n\t"
    "v_exp_f32 v32, v32\n\t"
    "v_mfma_f32_32x32x16_bf16 v[160:175], a[80:83], v[52:55], v[160:175]\n\t"
    "ds_read_b64_tr_b16 a[120:121], v100 offset:61440\n\t"
    "v_add_f32 v80, v80, v32\n\t"
    "v_exp_f32 v33, v33\n\t"
    "v_mfma_f32_32x32x16_bf16 v[176:191], a[84:87], v[52:55], v[176:191]\n\t"
    "ds_read_b64_tr_b16 a[122:123], v100 offset:63488\n\t"
    "ds_read_b128 a[56:59], v88 offset:40960\n\t"
    "v_cvt_pk_bf16_f32 v56, v32, v33\n\t"
    "v_add_f32 v80, v80, v33\n\t"
    "v_exp_f32 v34, v34\n\t"
    "v_mfma_f32_32x32x16_bf16 v[192:207], a[88:91], v[52:55], v[192:207]\n\t"
    "buffer_load_dwordx4 v110, s[48:51], s76 offen offset:3072 nt lds\n\t"
    "ds_read_b64_tr_b16 a[124:125], v101 offset:61440\n\t"
    "v_add_f32 v80, v80, v34\n\t"
    "v_exp_f32 v35, v35\n\t"
    "v_mfma_f32_32x32x16_bf16 v[208:223], a[92:95], v[52:55], v[208:223]\n\t"
    "ds_read_b64_tr_b16 a[126:127], v101 offset:63488\n\t"
    "ds_read_b128 a[60:63], v89 offset:40960\n\t"
    "v_cvt_pk_bf16_f32 v57, v34, v35\n\t"
    "v_add_f32 v80, v80, v35\n\t"
    "v_exp_f32 v36, v36\n\t"
    "s_waitcnt vmcnt(8)\n\t"
    "s_barrier\n\t"
    "v_mfma_f32_32x32x16_bf16 v[16:31], a[0:3], v[128:131], 0\n\t"
    "s_add_u32 s76, s75, 81920\n\t"
    "s_add_u32 m0, s75, 0\n\t"
    "ds_read_b64_tr_b16 a[64:65], v102 offset:16384\n\t"
    "v_add_f32 v80, v80, v36\n\t"
    "v_exp_f32 v37, v37\n\t"
    "v_mfma_f32_32x32x16_bf16 v[16:31], a[4:7], v[132:135], v[16:31]\n\t"
    "ds_read_b128 a[0:3], v90\n\t"
    "ds_read_b64_tr_b16 a[66:67], v102 offset:18432\n\t"
    "v_cvt_pk_bf16_f32 v58, v36, v37\n\t"
    "v_add_f32 v80, v80, v37\n\t"
    "v_exp_f32 v38, v38\n\t"
    "v_mfma_f32_32x32x16_bf16 v[16:31], a[8:11], v[136:139], v[16:31]\n\t"
    "buffer_load_dwordx4 v106, s[44:47], s76 offen nt lds\n\t"
    "ds_read_b64_tr_b16 a[68:69], v103 offset:16384\n\t"
    "v_add_f32 v80, v80, v38\n\t"
    "v_exp_f32 v39, v39\n\t"
    "v_mfma_f32_32x32x16_bf16 v[16:31], a[12:15], v[140:143], v[16:31]\n\t"
    "ds_read_b128 a[4:7], v91\n\t"
    "ds_read_b64_tr_b16 a[70:71], v103 offset:18432\n\t"
    "v_cvt_pk_bf16_f32 v59, v38, v39\n\t"
    "v_add_f32 v80, v80, v39\n\t"
    "v_exp_f32 v40, v40\n\t"
    "v_mfma_f32_32x32x16_bf16 v[16:31], a[16:19], v[144:147], v[16:31]\n\t"
    "ds_read_b64_tr_b16 a[72:73], v104 offset:16384\n\t"
    "v_add_f32 v80, v80, v40\n\t"
    "v_exp_f32 v41, v41\n\t"
    "v_mfma_f32_32x32x16_bf16 v[16:31], a[20:23], v[148:151], v[16:31]\n\t"
    "ds_read_b128 a[8:11], v92\n\t"
    "ds_read_b64_tr_b16 a[74:75], v104 offset:18432\n\t"
    "v_cvt_pk_bf16_f32 v60, v40, v41\n\t"
    "v_add_f32 v80, v80, v41\n\t"
    "v_exp_f32 v42, v42\n\t"
    "v_mfma_f32_32x32x16_bf16 v[16:31], a[24:27], v[152:155], v[16:31]\n\t"
    "buffer_load_dwordx4 v107, s[44:47], s76 offen offset:1024 nt lds\n\t"
    "ds_read_b64_tr_b16 a[76:77], v105 offset:16384\n\t"
    "v_add_f32 v80, v80, v42\n\t"
    "v_exp_f32 v43, v43\n\t"
    "v_mfma_f32_32x32x16_bf16 v[16:31], a[28:31], v[156:159], v[16:31]\n\t"
    "ds_read_b128 a[12:15], v93\n\t"
    "ds_read_b64_tr_b16 a[78:79], v105 offset:18432\n\t"
    "v_cvt_pk_bf16_f32 v61, v42, v43\n\t"
    "v_add_f32 v80, v80, v43\n\t"
    "v_exp_f32 v44, v44\n\t"
    "s_waitcnt lgkmcnt(13)\n\t"
    "v_mfma_f32_32x32x16_bf16 v[160:175], a[96:99], v[56:59], v[160:175]\n\t"
    "ds_read_b64_tr_b16 a[80:81], v102 offset:20480\n\t"
    "v_add_f32 v80, v80, v44\n\t"
    "v_exp_f32 v45, v45\n\t"
    "v_mfma_f32_32x32x16_bf16 v[176:191], a[100:103], v[56:59], v[176:191]\n\t"
    "ds_read_b128 a[16:19], v94\n\t"
    "ds_read_b64_tr_b16 a[82:83], v102 offset:22528\n\t"
    "v_cvt_pk_bf16_f32 v62, v44, v45\n\t"
    "v_add_f32 v80, v80, v45\n\t"
    "v_exp_f32 v46, v46\n\t"
    "v_mfma_f32_32x32x16_bf16 v[192:207], a[104:107], v[56:59], v[192:207]\n\t"
    "buffer_load_dwordx4 v108, s[44:47], s76 offen offset:2048 nt lds\n\t"
    "ds_read_b64_tr_b16 a[84:85], v103 offset:20480\n\t"
    "v_add_f32 v80, v80, v46\n\t"
    "v_exp_f32 v47, v47\n\t"
    "v_mfma_f32_32x32x16_bf16 v[208:223], a[108:111], v[56:59], v[208:223]\n\t"
    "ds_read_b128 a[20:23], v95\n\t"
    "ds_read_b64_tr_b16 a[86:87], v103 offset:22528\n\t"
    "v_cvt_pk_bf16_f32 v63, v46, v47\n\t"
    "v_add_f32 v80, v80, v47\n\t"
    "v_exp_f32 v16, v16\n\t"
    "v_mfma_f32_32x32x16_bf16 v[160:175], a[112:115], v[60:63], v[160:175]\n\t"
    "ds_read_b64_tr_b16 a[88:89], v104 offset:20480\n\t"
    "v_add_f32 v80, v80, v16\n\t"
    "v_exp_f32 v17, v17\n\t"
    "v_mfma_f32_32x32x16_bf16 v[176:191], a[116:119], v[60:63], v[176:191]\n\t"
    "ds_read_b128 a[24:27], v96\n\t"
    "ds_read_b64_tr_b16 a[90:91], v104 offset:22528\n\t"
    "v_cvt_pk_bf16_f32 v48, v16, v17\n\t"
    "v_add_f32 v80, v80, v17\n\t"
    "v_exp_f32 v18, v18\n\t"
    "v_mfma_f32_32x32x16_bf16 v[192:207], a[120:123], v[60:63], v[192:207]\n\t"
    "buffer_load_dwordx4 v109, s[44:47], s76 offen offset:3072 nt lds\n\t"
    "ds_read_b64_tr_b16 a[92:93], v105 offset:20480\n\t"
    "v_add_f32 v80, v80, v18\n\t"
    "v_exp_f32 v19, v19\n\t"
    "v_mfma_f32_32x32x16_bf16 v[208:223], a[124:127], v[60:63], v[208:223]\n\t"
    "ds_read_b128 a[28:31], v97\n\t"
    "ds_read_b64_tr_b16 a[94:95], v105 offset:22528\n\t"
    "v_cvt_pk_bf16_f32 v49, v18, v19\n\t"
    "v_add_f32 v80, v80, v19\n\t"
    "v_exp_f32 v20, v20\n\t"
    "s_waitcnt lgkmcnt(15)\n\t"
    "v_mfma_f32_32x32x16_bf16 v[32:47], a[32:35], v[128:131], 0\n\t"
    "s_add_u32 s76, s75, 65536\n\t"
    "s_add_u32 m0, s75, 16384\n\t"
    "ds_read_b64_tr_b16 a[96:97], v102 offset:24576\n\t"
    "v_add_f32 v80, v80, v20\n\t"
    "v_exp_f32 v21, v21\n\t"
    "v_mfma_f32_32x32x16_bf16 v[32:47], a[36:39], v[132:135], v[32:47]\n\t"
    "ds_read_b64_tr_b16 a[98:99], v102 offset:26624\n\t"
    "ds_read_b128 a[32:35], v90 offset:8192\n\t"
    "v_cvt_pk_bf16_f32 v50, v20, v21\n\t"
    "v_add_f32 v80, v80, v21\n\t"
    "v_exp_f32 v22, v22\n\t"
    "v_mfma_f32_32x32x16_bf16 v[32:47], a[40:43], v[136:139], v[32:47]\n\t"
    "buffer_load_dwordx4 v110, s[48:51], s76 offen nt lds\n\t"
    "ds_read_b64_tr_b16 a[100:101], v103 offset:24576\n\t"
    "v_add_f32 v80, v80, v22\n\t"
    "v_exp_f32 v23, v23\n\t"
    "v_mfma_f32_32x32x16_bf16 v[32:47], a[44:47], v[140:143], v[32:47]\n\t"
    "ds_read_b64_tr_b16 a[102:103], v103 offset:26624\n\t"
    "ds_read_b128 a[36:39], v91 offset:8192\n\t"
    "v_cvt_pk_bf16_f32 v51, v22, v23\n\t"
    "v_add_f32 v80, v80, v23\n\t"
    "v_exp_f32 v24, v24\n\t"
    "v_mfma_f32_32x32x16_bf16 v[32:47], a[48:51], v[144:147], v[32:47]\n\t"
    "ds_read_b64_tr_b16 a[104:105], v104 offset:24576\n\t"
    "v_add_f32 v80, v80, v24\n\t"
    "v_exp_f32 v25, v25\n\t"
    "v_mfma_f32_32x32x16_bf16 v[32:47], a[52:55], v[148:151], v[32:47]\n\t"
    "ds_read_b64_tr_b16 a[106:107], v104 offset:26624\n\t"
    "ds_read_b128 a[40:43], v92 offset:8192\n\t"
    "v_cvt_pk_bf16_f32 v52, v24, v25\n\t"
    "v_add_f32 v80, v80, v25\n\t"
    "v_exp_f32 v26, v26\n\t"
    "v_mfma_f32_32x32x16_bf16 v[32:47], a[56:59], v[152:155], v[32:47]\n\t"
    "buffer_load_dwordx4 v110, s[48:51], s76 offen offset:1024 nt lds\n\t"
    "ds_read_b64_tr_b16 a[108:109], v105 offset:24576\n\t"
    "v_add_f32 v80, v80, v26\n\t"
    "v_exp_f32 v27, v27\n\t"
    "v_mfma_f32_32x32x16_bf16 v[32:47], a[60:63], v[156:159], v[32:47]\n\t"
    "ds_read_b64_tr_b16 a[110:111], v105 offset:26624\n\t"
    "ds_read_b128 a[44:47], v93 offset:8192\n\t"
    "v_cvt_pk_bf16_f32 v53, v26, v27\n\t"
    "v_add_f32 v80, v80, v27\n\t"
    "v_exp_f32 v28, v28\n\t"
    "s_waitcnt lgkmcnt(12)\n\t"
    "v_mfma_f32_32x32x16_bf16 v[160:175], a[64:67], v[48:51], v[160:175]\n\t"
    "ds_read_b64_tr_b16 a[112:113], v102 offset:28672\n\t"
    "v_add_f32 v80, v80, v28\n\t"
    "v_exp_f32 v29, v29\n\t"
    "v_mfma_f32_32x32x16_bf16 v[176:191], a[68:71], v[48:51], v[176:191]\n\t"
    "ds_read_b64_tr_b16 a[114:115], v102 offset:30720\n\t"
    "ds_read_b128 a[48:51], v94 offset:8192\n\t"
    "v_cvt_pk_bf16_f32 v54, v28, v29\n\t"
    "v_add_f32 v80, v80, v29\n\t"
    "v_exp_f32 v30, v30\n\t"
    "v_mfma_f32_32x32x16_bf16 v[192:207], a[72:75], v[48:51], v[192:207]\n\t"
    "buffer_load_dwordx4 v110, s[48:51], s76 offen offset:2048 nt lds\n\t"
    "ds_read_b64_tr_b16 a[116:117], v103 offset:28672\n\t"
    "v_add_f32 v80, v80, v30\n\t"
    "v_exp_f32 v31, v31\n\t"
    "v_mfma_f32_32x32x16_bf16 v[208:223], a[76:79], v[48:51], v[208:223]\n\t"
    "ds_read_b64_tr_b16 a[118:119], v103 offset:30720\n\t"
    "ds_read_b128 a[52:55], v95 offset:8192\n\t"
    "v_cvt_pk_bf16_f32 v55, v30, v31\n\t"
    "v_add_f32 v80, v80, v31\n\t"
    "v_exp_f32 v32, v32\n\t"
    "v_mfma_f32_32x32x16_bf16 v[160:175], a[80:83], v[52:55], v[160:175]\n\t"
    "ds_read_b64_tr_b16 a[120:121], v104 offset:28672\n\t"
    "v_add_f32 v80, v80, v32\n\t"
    "v_exp_f32 v33, v33\n\t"
    "v_mfma_f32_32x32x16_bf16 v[176:191], a[84:87], v[52:55], v[176:191]\n\t"
    "ds_read_b64_tr_b16 a[122:123], v104 offset:30720\n\t"
    "ds_read_b128 a[56:59], v96 offset:8192\n\t"
    "v_cvt_pk_bf16_f32 v56, v32, v33\n\t"
    "v_add_f32 v80, v80, v33\n\t"
    "v_exp_f32 v34, v34\n\t"
    "v_mfma_f32_32x32x16_bf16 v[192:207], a[88:91], v[52:55], v[192:207]\n\t"
    "buffer_load_dwordx4 v110, s[48:51], s76 offen offset:3072 nt lds\n\t"
    "ds_read_b64_tr_b16 a[124:125], v105 offset:28672\n\t"
    "v_add_f32 v80, v80, v34\n\t"
    "v_exp_f32 v35, v35\n\t"
    "v_mfma_f32_32x32x16_bf16 v[208:223], a[92:95], v[52:55], v[208:223]\n\t"
    "ds_read_b64_tr_b16 a[126:127], v105 offset:30720\n\t"
    "ds_read_b128 a[60:63], v97 offset:8192\n\t"
    "v_cvt_pk_bf16_f32 v57, v34, v35\n\t"
    "v_add_f32 v80, v80, v35\n\t"
    "v_exp_f32 v36, v36\n\t"
    "s_waitcnt vmcnt(8)\n\t"
    "s_barrier\n\t"
    "v_mfma_f32_32x32x16_bf16 v[16:31], a[0:3], v[128:131], 0\n\t"
    "s_add_u32 s76, s75, 98304\n\t"
    "s_add_u32 m0, s75, 32768\n\t"
    "ds_read_b64_tr_b16 a[64:65], v102 offset:49152\n\t"
    "v_add_f32 v80, v80, v36\n\t"
    "v_exp_f32 v37, v37\n\t"
    "v_mfma_f32_32x32x16_bf16 v[16:31], a[4:7], v[132:135], v[16:31]\n\t"
    "ds_read_b128 a[0:3], v90 offset:32768\n\t"
    "ds_read_b64_tr_b16 a[66:67], v102 offset:51200\n\t"
    "v_cvt_pk_bf16_f32 v58, v36, v37\n\t"
    "v_add_f32 v80, v80, v37\n\t"
    "v_exp_f32 v38, v38\n\t"
    "v_mfma_f32_32x32x16_bf16 v[16:31], a[8:11], v[136:139], v[16:31]\n\t"
    "buffer_load_dwordx4 v106, s[44:47], s76 offen nt lds\n\t"
    "ds_read_b64_tr_b16 a[68:69], v103 offset:49152\n\t"
    "v_add_f32 v80, v80, v38\n\t"
    "v_exp_f32 v39, v39\n\t"
    "v_mfma_f32_32x32x16_bf16 v[16:31], a[12:15], v[140:143], v[16:31]\n\t"
    "ds_read_b128 a[4:7], v91 offset:32768\n\t"
    "ds_read_b64_tr_b16 a[70:71], v103 offset:51200\n\t"
    "v_cvt_pk_bf16_f32 v59, v38, v39\n\t"
    "v_add_f32 v80, v80, v39\n\t"
    "v_exp_f32 v40, v40\n\t"
    "v_mfma_f32_32x32x16_bf16 v[16:31], a[16:19], v[144:147], v[16:31]\n\t"
    "ds_read_b64_tr_b16 a[72:73], v104 offset:49152\n\t"
    "v_add_f32 v80, v80, v40\n\t"
    "v_exp_f32 v41, v41\n\t"
    "v_mfma_f32_32x32x16_bf16 v[16:31], a[20:23], v[148:151], v[16:31]\n\t"
    "ds_read_b128 a[8:11], v92 offset:32768\n\t"
    "ds_read_b64_tr_b16 a[74:75], v104 offset:51200\n\t"
    "v_cvt_pk_bf16_f32 v60, v40, v41\n\t"
    "v_add_f32 v80, v80, v41\n\t"
    "v_exp_f32 v42, v42\n\t"
    "v_mfma_f32_32x32x16_bf16 v[16:31], a[24:27], v[152:155], v[16:31]\n\t"
    "buffer_load_dwordx4 v107, s[44:47], s76 offen offset:1024 nt lds\n\t"
    "ds_read_b64_tr_b16 a[76:77], v105 offset:49152\n\t"
    "v_add_f32 v80, v80, v42\n\t"
    "v_exp_f32 v43, v43\n\t"
    "v_mfma_f32_32x32x16_bf16 v[16:31], a[28:31], v[156:159], v[16:31]\n\t"
    "ds_read_b128 a[12:15], v93 offset:32768\n\t"
    "ds_read_b64_tr_b16 a[78:79], v105 offset:51200\n\t"
    "v_cvt_pk_bf16_f32 v61, v42, v43\n\t"
    "v_add_f32 v80, v80, v43\n\t"
    "v_exp_f32 v44, v44\n\t"
    "s_waitcnt lgkmcnt(13)\n\t"
    "v_mfma_f32_32x32x16_bf16 v[160:175], a[96:99], v[56:59], v[160:175]\n\t"
    "ds_read_b64_tr_b16 a[80:81], v102 offset:53248\n\t"
    "v_add_f32 v80, v80, v44\n\t"
    "v_exp_f32 v45, v45\n\t"
    "v_mfma_f32_32x32x16_bf16 v[176:191], a[100:103], v[56:59], v[176:191]\n\t"
    "ds_read_b128 a[16:19], v94 offset:32768\n\t"
    "ds_read_b64_tr_b16 a[82:83], v102 offset:55296\n\t"
    "v_cvt_pk_bf16_f32 v62, v44, v45\n\t"
    "v_add_f32 v80, v80, v45\n\t"
    "v_exp_f32 v46, v46\n\t"
    "v_mfma_f32_32x32x16_bf16 v[192:207], a[104:107], v[56:59], v[192:207]\n\t"
    "buffer_load_dwordx4 v108, s[44:47], s76 offen offset:2048 nt lds\n\t"
    "ds_read_b64_tr_b16 a[84:85], v103 offset:53248\n\t"
    "v_add_f32 v80, v80, v46\n\t"
    "v_exp_f32 v47, v47\n\t"
    "v_mfma_f32_32x32x16_bf16 v[208:223], a[108:111], v[56:59], v[208:223]\n\t"
    "ds_read_b128 a[20:23], v95 offset:32768\n\t"
    "ds_read_b64_tr_b16 a[86:87], v103 offset:55296\n\t"
    "v_cvt_pk_bf16_f32 v63, v46, v47\n\t"
    "v_add_f32 v80, v80, v47\n\t"
    "v_exp_f32 v16, v16\n\t"
    "v_mfma_f32_32x32x16_bf16 v[160:175], a[112:115], v[60:63], v[160:175]\n\t"
    "ds_read_b64_tr_b16 a[88:89], v104 offset:53248\n\t"
    "v_add_f32 v80, v80, v16\n\t"
    "v_exp_f32 v17, v17\n\t"
    "v_mfma_f32_32x32x16_bf16 v[176:191], a[116:119], v[60:63], v[176:191]\n\t"
    "ds_read_b128 a[24:27], v96 offset:32768\n\t"
    "ds_read_b64_tr_b16 a[90:91], v104 offset:55296\n\t"
    "v_cvt_pk_bf16_f32 v48, v16, v17\n\t"
    "v_add_f32 v80, v80, v17\n\t"
    "v_exp_f32 v18, v18\n\t"
    "v_mfma_f32_32x32x16_bf16 v[192:207], a[120:123], v[60:63], v[192:207]\n\t"
    "buffer_load_dwordx4 v109, s[44:47], s76 offen offset:3072 nt lds\n\t"
    "ds_read_b64_tr_b16 a[92:93], v105 offset:53248\n\t"
    "v_add_f32 v80, v80, v18\n\t"
    "v_exp_f32 v19, v19\n\t"
    "v_mfma_f32_32x32x16_bf16 v[208:223], a[124:127], v[60:63], v[208:223]\n\t"
    "ds_read_b128 a[28:31], v97 offset:32768\n\t"
    "ds_read_b64_tr_b16 a[94:95], v105 offset:55296\n\t"
    "v_cvt_pk_bf16_f32 v49, v18, v19\n\t"
    "v_add_f32 v80, v80, v19\n\t"
    "v_exp_f32 v20, v20\n\t"
    "s_waitcnt lgkmcnt(15)\n\t"
    "v_mfma_f32_32x32x16_bf16 v[32:47], a[32:35], v[128:131], 0\n\t"
    "s_add_u32 s76, s75, 81920\n\t"
    "s_add_u32 m0, s75, 49152\n\t"
    "ds_read_b64_tr_b16 a[96:97], v102 offset:57344\n\t"
    "v_add_f32 v80, v80, v20\n\t"
    "v_exp_f32 v21, v21\n\t"
    "v_mfma_f32_32x32x16_bf16 v[32:47], a[36:39], v[132:135], v[32:47]\n\t"
    "ds_read_b64_tr_b16 a[98:99], v102 offset:59392\n\t"
    "ds_read_b128 a[32:35], v90 offset:40960\n\t"
    "v_cvt_pk_bf16_f32 v50, v20, v21\n\t"
    "v_add_f32 v80, v80, v21\n\t"
    "v_exp_f32 v22, v22\n\t"
    "v_mfma_f32_32x32x16_bf16 v[32:47], a[40:43], v[136:139], v[32:47]\n\t"
    "buffer_load_dwordx4 v110, s[48:51], s76 offen nt lds\n\t"
    "ds_read_b64_tr_b16 a[100:101], v103 offset:57344\n\t"
    "v_add_f32 v80, v80, v22\n\t"
    "v_exp_f32 v23, v23\n\t"
    "v_mfma_f32_32x32x16_bf16 v[32:47], a[44:47], v[140:143], v[32:47]\n\t"
    "ds_read_b64_tr_b16 a[102:103], v103 offset:59392\n\t"
    "ds_read_b128 a[36:39], v91 offset:40960\n\t"
    "v_cvt_pk_bf16_f32 v51, v22, v23\n\t"
    "v_add_f32 v80, v80, v23\n\t"
    "v_exp_f32 v24, v24\n\t"
    "v_mfma_f32_32x32x16_bf16 v[32:47], a[48:51], v[144:147], v[32:47]\n\t"
    "ds_read_b64_tr_b16 a[104:105], v104 offset:57344\n\t"
    "v_add_f32 v80, v80, v24\n\t"
    "v_exp_f32 v25, v25\n\t"
    "v_mfma_f32_32x32x16_bf16 v[32:47], a[52:55], v[148:151], v[32:47]\n\t"
    "ds_read_b64_tr_b16 a[106:107], v104 offset:59392\n\t"
    "ds_read_b128 a[40:43], v92 offset:40960\n\t"
    "v_cvt_pk_bf16_f32 v52, v24, v25\n\t"
    "v_add_f32 v80, v80, v25\n\t"
    "v_exp_f32 v26, v26\n\t"
    "v_mfma_f32_32x32x16_bf16 v[32:47], a[56:59], v[152:155], v[32:47]\n\t"
    "buffer_load_dwordx4 v110, s[48:51], s76 offen offset:1024 nt lds\n\t"
    "ds_read_b64_tr_b16 a[108:109], v105 offset:57344\n\t"
    "v_add_f32 v80, v80, v26\n\t"
    "v_exp_f32 v27, v27\n\t"
    "v_mfma_f32_32x32x16_bf16 v[32:47], a[60:63], v[156:159], v[32:47]\n\t"
    "ds_read_b64_tr_b16 a[110:111], v105 offset:59392\n\t"
    "ds_read_b128 a[44:47], v93 offset:40960\n\t"
    "v_cvt_pk_bf16_f32 v53, v26, v27\n\t"
    "v_add_f32 v80, v80, v27\n\t"
    "v_exp_f32 v28, v28\n\t"
    "s_waitcnt lgkmcnt(12)\n\t"
    "v_mfma_f32_32x32x16_bf16 v[160:175], a[64:67], v[48:51], v[160:175]\n\t"
    "ds_read_b64_tr_b16 a[112:113], v102 offset:61440\n\t"
    "v_add_f32 v80, v80, v28\n\t"
    "v_exp_f32 v29, v29\n\t"
    "v_mfma_f32_32x32x16_bf16 v[176:191], a[68:71], v[48:51], v[176:191]\n\t"
    "ds_read_b64_tr_b16 a[114:115], v102 offset:63488\n\t"
    "ds_read_b128 a[48:51], v94 offset:40960\n\t"
    "v_cvt_pk_bf16_f32 v54, v28, v29\n\t"
    "v_add_f32 v80, v80, v29\n\t"
    "v_exp_f32 v30, v30\n\t"
    "v_mfma_f32_32x32x16_bf16 v[192:207], a[72:75], v[48:51], v[192:207]\n\t"
    "buffer_load_dwordx4 v110, s[48:51], s76 offen offset:2048 nt lds\n\t"
    "ds_read_b64_tr_b16 a[116:117], v103 offset:61440\n\t"
    "v_add_f32 v80, v80, v30\n\t"
    "v_exp_f32 v31, v31\n\t"
    "v_mfma_f32_32x32x16_bf16 v[208:223], a[76:79], v[48:51], v[208:223]\n\t"
    "ds_read_b64_tr_b16 a[118:119], v103 offset:63488\n\t"
    "ds_read_b128 a[52:55], v95 offset:40960\n\t"
    "v_cvt_pk_bf16_f32 v55, v30, v31\n\t"
    "v_add_f32 v80, v80, v31\n\t"
    "v_exp_f32 v32, v32\n\t"
    "v_mfma_f32_32x32x16_bf16 v[160:175], a[80:83], v[52:55], v[160:175]\n\t"
    "ds_read_b64_tr_b16 a[120:121], v104 offset:61440\n\t"
    "v_add_f32 v80, v80, v32\n\t"
    "v_exp_f32 v33, v33\n\t"
    "v_mfma_f32_32x32x16_bf16 v[176:191], a[84:87], v[52:55], v[176:191]\n\t"
    "ds_read_b64_tr_b16 a[122:123], v104 offset:63488\n\t"
    "ds_read_b128 a[56:59], v96 offset:40960\n\t"
    "v_cvt_pk_bf16_f32 v56, v32, v33\n\t"
    "v_add_f32 v80, v80, v33\n\t"
    "v_exp_f32 v34, v34\n\t"
    "v_mfma_f32_32x32x16_bf16 v[192:207], a[88:91], v[52:55], v[192:207]\n\t"
    "buffer_load_dwordx4 v110, s[48:51], s76 offen offset:3072 nt lds\n\t"
    "ds_read_b64_tr_b16 a[124:125], v105 offset:61440\n\t"
    "v_add_f32 v80, v80, v34\n\t"
    "v_exp_f32 v35, v35\n\t"
    "v_mfma_f32_32x32x16_bf16 v[208:223], a[92:95], v[52:55], v[208:223]\n\t"
    "ds_read_b64_tr_b16 a[126:127], v105 offset:63488\n\t"
    "ds_read_b128 a[60:63], v97 offset:40960\n\t"
    "v_cvt_pk_bf16_f32 v57, v34, v35\n\t"
    "v_add_f32 v80, v80, v35\n\t"
    "v_exp_f32 v36, v36\n\t"
    "s_waitcnt vmcnt(8)\n\t"
    "s_barrier\n\t"
    "v_mfma_f32_32x32x16_bf16 v[16:31], a[0:3], v[128:131], 0\n\t"
    "s_add_u32 s76, s75, 114688\n\t"
    "s_add_u32 m0, s75, 65536\n\t"
    "ds_read_b64_tr_b16 a[64:65], v98 offset:16384\n\t"
    "v_add_f32 v80, v80, v36\n\t"
    "v_exp_f32 v37, v37\n\t"
    "v_mfma_f32_32x32x16_bf16 v[16:31], a[4:7], v[132:135], v[16:31]\n\t"
    "ds_read_b128 a[0:3], v82\n\t"
    "ds_read_b64_tr_b16 a[66:67], v98 offset:18432\n\t"
    "v_cvt_pk_bf16_f32 v58, v36, v37\n\t"
    "v_add_f32 v80, v80, v37\n\t"
    "v_exp_f32 v38, v38\n\t"
    "v_mfma_f32_32x32x16_bf16 v[16:31], a[8:11], v[136:139], v[16:31]\n\t"
    "buffer_load_dwordx4 v106, s[44:47], s76 offen nt lds\n\t"
    "ds_read_b64_tr_b16 a[68:69], v99 offset:16384\n\t"
    "v_add_f32 v80, v80, v38\n\t"
    "v_exp_f32 v39, v39\n\t"
    "v_mfma_f32_32x32x16_bf16 v[16:31], a[12:15], v[140:143], v[16:31]\n\t"
    "ds_read_b128 a[4:7], v83\n\t"
    "ds_read_b64_tr_b16 a[70:71], v99 offset:18432\n\t"
    "v_cvt_pk_bf16_f32 v59, v38, v39\n\t"
    "v_add_f32 v80, v80, v39\n\t"
    "v_exp_f32 v40, v40\n\t"
    "v_mfma_f32_32x32x16_bf16 v[16:31], a[16:19], v[144:147], v[16:31]\n\t"
    "ds_read_b64_tr_b16 a[72:73], v100 offset:16384\n\t"
    "v_add_f32 v80, v80, v40\n\t"
    "v_exp_f32 v41, v41\n\t"
    "v_mfma_f32_32x32x16_bf16 v[16:31], a[20:23], v[148:151], v[16:31]\n\t"
    "ds_read_b128 a[8:11], v84\n\t"
    "ds_read_b64_tr_b16 a[74:75], v100 offset:18432\n\t"
    "v_cvt_pk_bf16_f32 v60, v40, v41\n\t"
    "v_add_f32 v80, v80, v41\n\t"
    "v_exp_f32 v42, v42\n\t"
    "v_mfma_f32_32x32x16_bf16 v[16:31], a[24:27], v[152:155], v[16:31]\n\t"
    "buffer_load_dwordx4 v107, s[44:47], s76 offen offset:1024 nt lds\n\t"
    "ds_read_b64_tr_b16 a[76:77], v101 offset:16384\n\t"
    "v_add_f32 v80, v80, v42\n\t"
    "v_exp_f32 v43, v43\n\t"
    "v_mfma_f32_32x32x16_bf16 v[16:31], a[28:31], v[156:159], v[16:31]\n\t"
    "ds_read_b128 a[12:15], v85\n\t"
    "ds_read_b64_tr_b16 a[78:79], v101 offset:18432\n\t"
    "v_cvt_pk_bf16_f32 v61, v42, v43\n\t"
    "v_add_f32 v80, v80, v43\n\t"
    "v_exp_f32 v44, v44\n\t"
    "s_waitcnt lgkmcnt(13)\n\t"
    "v_mfma_f32_32x32x16_bf16 v[160:175], a[96:99], v[56:59], v[160:175]\n\t"
    "ds_read_b64_tr_b16 a[80:81], v98 offset:20480\n\t"
    "v_add_f32 v80, v80, v44\n\t"
    "v_exp_f32 v45, v45\n\t"
    "v_mfma_f32_32x32x16_bf16 v[176:191], a[100:103], v[56:59], v[176:191]\n\t"
    "ds_read_b128 a[16:19], v86\n\t"
    "ds_read_b64_tr_b16 a[82:83], v98 offset:22528\n\t"
    "v_cvt_pk_bf16_f32 v62, v44, v45\n\t"
    "v_add_f32 v80, v80, v45\n\t"
    "v_exp_f32 v46, v46\n\t"
    "v_mfma_f32_32x32x16_bf16 v[192:207], a[104:107], v[56:59], v[192:207]\n\t"
    "buffer_load_dwordx4 v108, s[44:47], s76 offen offset:2048 nt lds\n\t"
    "ds_read_b64_tr_b16 a[84:85], v99 offset:20480\n\t"
    "v_add_f32 v80, v80, v46\n\t"
    "v_exp_f32 v47, v47\n\t"
    "v_mfma_f32_32x32x16_bf16 v[208:223], a[108:111], v[56:59], v[208:223]\n\t"
    "ds_read_b128 a[20:23], v87\n\t"
    "ds_read_b64_tr_b16 a[86:87], v99 offset:22528\n\t"
    "v_cvt_pk_bf16_f32 v63, v46, v47\n\t"
    "v_add_f32 v80, v80, v47\n\t"
    "v_exp_f32 v16, v16\n\t"
    "v_mfma_f32_32x32x16_bf16 v[160:175], a[112:115], v[60:63], v[160:175]\n\t"
    "ds_read_b64_tr_b16 a[88:89], v100 offset:20480\n\t"
    "v_add_f32 v80, v80, v16\n\t"
    "v_exp_f32 v17, v17\n\t"
    "v_mfma_f32_32x32x16_bf16 v[176:191], a[116:119], v[60:63], v[176:191]\n\t"
    "ds_read_b128 a[24:27], v88\n\t"
    "ds_read_b64_tr_b16 a[90:91], v100 offset:22528\n\t"
    "v_cvt_pk_bf16_f32 v48, v16, v17\n\t"
    "v_add_f32 v80, v80, v17\n\t"
    "v_exp_f32 v18, v18\n\t"
    "v_mfma_f32_32x32x16_bf16 v[192:207], a[120:123], v[60:63], v[192:207]\n\t"
    "buffer_load_dwordx4 v109, s[44:47], s76 offen offset:3072 nt lds\n\t"
    "ds_read_b64_tr_b16 a[92:93], v101 offset:20480\n\t"
    "v_add_f32 v80, v80, v18\n\t"
    "v_exp_f32 v19, v19\n\t"
    "v_mfma_f32_32x32x16_bf16 v[208:223], a[124:127], v[60:63], v[208:223]\n\t"
    "ds_read_b128 a[28:31], v89\n\t"
    "ds_read_b64_tr_b16 a[94:95], v101 offset:22528\n\t"
    "v_cvt_pk_bf16_f32 v49, v18, v19\n\t"
    "v_add_f32 v80, v80, v19\n\t"
    "v_exp_f32 v20, v20\n\t"
    "s_waitcnt lgkmcnt(15)\n\t"
    "v_mfma_f32_32x32x16_bf16 v[32:47], a[32:35], v[128:131], 0\n\t"
    "s_add_u32 s76, s75, 98304\n\t"
    "s_add_u32 m0, s75, 81920\n\t"
    "ds_read_b64_tr_b16 a[96:97], v98 offset:24576\n\t"
    "v_add_f32 v80, v80, v20\n\t"
    "v_exp_f32 v21, v21\n\t"
    "v_mfma_f32_32x32x16_bf16 v[32:47], a[36:39], v[132:135], v[32:47]\n\t"
    "ds_read_b64_tr_b16 a[98:99], v98 offset:26624\n\t"
    "ds_read_b128 a[32:35], v82 offset:8192\n\t"
    "v_cvt_pk_bf16_f32 v50, v20, v21\n\t"
    "v_add_f32 v80, v80, v21\n\t"
    "v_exp_f32 v22, v22\n\t"
    "v_mfma_f32_32x32x16_bf16 v[32:47], a[40:43], v[136:139], v[32:47]\n\t"
    "buffer_load_dwordx4 v110, s[48:51], s76 offen nt lds\n\t"
    "ds_read_b64_tr_b16 a[100:101], v99 offset:24576\n\t"
    "v_add_f32 v80, v80, v22\n\t"
    "v_exp_f32 v23, v23\n\t"
    "v_mfma_f32_32x32x16_bf16 v[32:47], a[44:47], v[140:143], v[32:47]\n\t"
    "ds_read_b64_tr_b16 a[102:103], v99 offset:26624\n\t"
    "ds_read_b128 a[36:39], v83 offset:8192\n\t"
    "v_cvt_pk_bf16_f32 v51, v22, v23\n\t"
    "v_add_f32 v80, v80, v23\n\t"
    "v_exp_f32 v24, v24\n\t"
    "v_mfma_f32_32x32x16_bf16 v[32:47], a[48:51], v[144:147], v[32:47]\n\t"
    "ds_read_b64_tr_b16 a[104:105], v100 offset:24576\n\t"
    "v_add_f32 v80, v80, v24\n\t"
    "v_exp_f32 v25, v25\n\t"
    "v_mfma_f32_32x32x16_bf16 v[32:47], a[52:55], v[148:151], v[32:47]\n\t"
    "ds_read_b64_tr_b16 a[106:107], v100 offset:26624\n\t"
    "ds_read_b128 a[40:43], v84 offset:8192\n\t"
    "v_cvt_pk_bf16_f32 v52, v24, v25\n\t"
    "v_add_f32 v80, v80, v25\n\t"
    "v_exp_f32 v26, v26\n\t"
    "v_mfma_f32_32x32x16_bf16 v[32:47], a[56:59], v[152:155], v[32:47]\n\t"
    "buffer_load_dwordx4 v110, s[48:51], s76 offen offset:1024 nt lds\n\t"
    "ds_read_b64_tr_b16 a[108:109], v101 offset:24576\n\t"
    "v_add_f32 v80, v80, v26\n\t"
    "v_exp_f32 v27, v27\n\t"
    "v_mfma_f32_32x32x16_bf16 v[32:47], a[60:63], v[156:159], v[32:47]\n\t"
    "ds_read_b64_tr_b16 a[110:111], v101 offset:26624\n\t"
    "ds_read_b128 a[44:47], v85 offset:8192\n\t"
    "v_cvt_pk_bf16_f32 v53, v26, v27\n\t"
    "v_add_f32 v80, v80, v27\n\t"
    "v_exp_f32 v28, v28\n\t"
    "s_waitcnt lgkmcnt(12)\n\t"
    "v_mfma_f32_32x32x16_bf16 v[160:175], a[64:67], v[48:51], v[160:175]\n\t"
    "ds_read_b64_tr_b16 a[112:113], v98 offset:28672\n\t"
    "v_add_f32 v80, v80, v28\n\t"
    "v_exp_f32 v29, v29\n\t"
    "v_mfma_f32_32x32x16_bf16 v[176:191], a[68:71], v[48:51], v[176:191]\n\t"
    "ds_read_b64_tr_b16 a[114:115], v98 offset:30720\n\t"
    "ds_read_b128 a[48:51], v86 offset:8192\n\t"
    "v_cvt_pk_bf16_f32 v54, v28, v29\n\t"
    "v_add_f32 v80, v80, v29\n\t"
    "v_exp_f32 v30, v30\n\t"
    "v_mfma_f32_32x32x16_bf16 v[192:207], a[72:75], v[48:51], v[192:207]\n\t"
    "buffer_load_dwordx4 v110, s[48:51], s76 offen offset:2048 nt lds\n\t"
    "ds_read_b64_tr_b16 a[116:117], v99 offset:28672\n\t"
    "v_add_f32 v80, v80, v30\n\t"
    "v_exp_f32 v31, v31\n\t"
    "v_mfma_f32_32x32x16_bf16 v[208:223], a[76:79], v[48:51], v[208:223]\n\t"
    "ds_read_b64_tr_b16 a[118:119], v99 offset:30720\n\t"
    "ds_read_b128 a[52:55], v87 offset:8192\n\t"
    "v_cvt_pk_bf16_f32 v55, v30, v31\n\t"
    "v_add_f32 v80, v80, v31\n\t"
    "v_exp_f32 v32, v32\n\t"
    "v_mfma_f32_32x32x16_bf16 v[160:175], a[80:83], v[52:55], v[160:175]\n\t"
    "ds_read_b64_tr_b16 a[120:121], v100 offset:28672\n\t"
    "v_add_f32 v80, v80, v32\n\t"
    "v_exp_f32 v33, v33\n\t"
    "v_mfma_f32_32x32x16_bf16 v[176:191], a[84:87], v[52:55], v[176:191]\n\t"
    "ds_read_b64_tr_b16 a[122:123], v100 offset:30720\n\t"
    "ds_read_b128 a[56:59], v88 offset:8192\n\t"
    "v_cvt_pk_bf16_f32 v56, v32, v33\n\t"
    "v_add_f32 v80, v80, v33\n\t"
    "v_exp_f32 v34, v34\n\t"
    "v_mfma_f32_32x32x16_bf16 v[192:207], a[88:91], v[52:55], v[192:207]\n\t"
    "buffer_load_dwordx4 v110, s[48:51], s76 offen offset:3072 nt lds\n\t"
    "ds_read_b64_tr_b16 a[124:125], v101 offset:28672\n\t"
    "v_add_f32 v80, v80, v34\n\t"
    "v_exp_f32 v35, v35\n\t"
    "v_mfma_f32_32x32x16_bf16 v[208:223], a[92:95], v[52:55], v[208:223]\n\t"
    "ds_read_b64_tr_b16 a[126:127], v101 offset:30720\n\t"
    "ds_read_b128 a[60:63], v89 offset:8192\n\t"
    "v_cvt_pk_bf16_f32 v57, v34, v35\n\t"
    "v_add_f32 v80, v80, v35\n\t"
    "v_exp_f32 v36, v36\n\t"
    "s_waitcnt vmcnt(8)\n\t"
    "s_barrier\n\t"
    "v_mfma_f32_32x32x16_bf16 v[16:31], a[0:3], v[128:131], 0\n\t"
    "s_add_u32 s76, s75, 131072\n\t"
    "s_add_u32 m0, s75, 98304\n\t"
    "ds_read_b64_tr_b16 a[64:65], v98 offset:49152\n\t"
    "v_add_f32 v80, v80, v36\n\t"
    "v_exp_f32 v37, v37\n\t"
    "v_mfma_f32_32x32x16_bf16 v[16:31], a[4:7], v[132:135], v[16:31]\n\t"
    "ds_read_b128 a[0:3], v82 offset:32768\n\t"
    "ds_read_b64_tr_b16 a[66:67], v98 offset:51200\n\t"
    "v_cvt_pk_bf16_f32 v58, v36, v37\n\t"
    "v_add_f32 v80, v80, v37\n\t"
    "v_exp_f32 v38, v38\n\t"
    "v_mfma_f32_32x32x16_bf16 v[16:31], a[8:11], v[136:139], v[16:31]\n\t"
    "buffer_load_dwordx4 v106, s[44:47], s76 offen nt lds\n\t"
    "ds_read_b64_tr_b16 a[68:69], v99 offset:49152\n\t"
    "v_add_f32 v80, v80, v38\n\t"
    "v_exp_f32 v39, v39\n\t"
    "v_mfma_f32_32x32x16_bf16 v[16:31], a[12:15], v[140:143], v[16:31]\n\t"
    "ds_read_b128 a[4:7], v83 offset:32768\n\t"
    "ds_read_b64_tr_b16 a[70:71], v99 offset:51200\n\t"
    "v_cvt_pk_bf16_f32 v59, v38, v39\n\t"
    "v_add_f32 v80, v80, v39\n\t"
    "v_exp_f32 v40, v40\n\t"
    "v_mfma_f32_32x32x16_bf16 v[16:31], a[16:19], v[144:147], v[16:31]\n\t"
    "ds_read_b64_tr_b16 a[72:73], v100 offset:49152\n\t"
    "v_add_f32 v80, v80, v40\n\t"
    "v_exp_f32 v41, v41\n\t"
    "v_mfma_f32_32x32x16_bf16 v[16:31], a[20:23], v[148:151], v[16:31]\n\t"
    "ds_read_b128 a[8:11], v84 offset:32768\n\t"
    "ds_read_b64_tr_b16 a[74:75], v100 offset:51200\n\t"
    "v_cvt_pk_bf16_f32 v60, v40, v41\n\t"
    "v_add_f32 v80, v80, v41\n\t"
    "v_exp_f32 v42, v42\n\t"
    "v_mfma_f32_32x32x16_bf16 v[16:31], a[24:27], v[152:155], v[16:31]\n\t"
    "buffer_load_dwordx4 v107, s[44:47], s76 offen offset:1024 nt lds\n\t"
    "ds_read_b64_tr_b16 a[76:77], v101 offset:49152\n\t"
    "v_add_f32 v80, v80, v42\n\t"
    "v_exp_f32 v43, v43\n\t"
    "v_mfma_f32_32x32x16_bf16 v[16:31], a[28:31], v[156:159], v[16:31]\n\t"
    "ds_read_b128 a[12:15], v85 offset:32768\n\t"
    "ds_read_b64_tr_b16 a[78:79], v101 offset:51200\n\t"
    "v_cvt_pk_bf16_f32 v61, v42, v43\n\t"
    "v_add_f32 v80, v80, v43\n\t"
    "v_exp_f32 v44, v44\n\t"
    "s_waitcnt lgkmcnt(13)\n\t"
    "v_mfma_f32_32x32x16_bf16 v[160:175], a[96:99], v[56:59], v[160:175]\n\t"
    "ds_read_b64_tr_b16 a[80:81], v98 offset:53248\n\t"
    "v_add_f32 v80, v80, v44\n\t"
    "v_exp_f32 v45, v45\n\t"
    "v_mfma_f32_32x32x16_bf16 v[176:191], a[100:103], v[56:59], v[176:191]\n\t"
    "ds_read_b128 a[16:19], v86 offset:32768\n\t"
    "ds_read_b64_tr_b16 a[82:83], v98 offset:55296\n\t"
    "v_cvt_pk_bf16_f32 v62, v44, v45\n\t"
    "v_add_f32 v80, v80, v45\n\t"
    "v_exp_f32 v46, v46\n\t"
    "v_mfma_f32_32x32x16_bf16 v[192:207], a[104:107], v[56:59], v[192:207]\n\t"
    "buffer_load_dwordx4 v108, s[44:47], s76 offen offset:2048 nt lds\n\t"
    "ds_read_b64_tr_b16 a[84:85], v99 offset:53248\n\t"
    "v_add_f32 v80, v80, v46\n\t"
    "v_exp_f32 v47, v47\n\t"
    "v_mfma_f32_32x32x16_bf16 v[208:223], a[108:111], v[56:59], v[208:223]\n\t"
    "ds_read_b128 a[20:23], v87 offset:32768\n\t"
    "ds_read_b64_tr_b16 a[86:87], v99 offset:55296\n\t"
    "v_cvt_pk_bf16_f32 v63, v46, v47\n\t"
    "v_add_f32 v80, v80, v47\n\t"
    "v_exp_f32 v16, v16\n\t"
    "v_mfma_f32_32x32x16_bf16 v[160:175], a[112:115], v[60:63], v[160:175]\n\t"
    "ds_read_b64_tr_b16 a[88:89], v100 offset:53248\n\t"
    "v_add_f32 v80, v80, v16\n\t"
    "v_exp_f32 v17, v17\n\t"
    "v_mfma_f32_32x32x16_bf16 v[176:191], a[116:119], v[60:63], v[176:191]\n\t"
    "ds_read_b128 a[24:27], v88 offset:32768\n\t"
    "ds_read_b64_tr_b16 a[90:91], v100 offset:55296\n\t"
    "v_cvt_pk_bf16_f32 v48, v16, v17\n\t"
    "v_add_f32 v80, v80, v17\n\t"
    "v_exp_f32 v18, v18\n\t"
    "v_mfma_f32_32x32x16_bf16 v[192:207], a[120:123], v[60:63], v[192:207]\n\t"
    "buffer_load_dwordx4 v109, s[44:47], s76 offen offset:3072 nt lds\n\t"
    "ds_read_b64_tr_b16 a[92:93], v101 offset:53248\n\t"
    "v_add_f32 v80, v80, v18\n\t"
    "v_exp_f32 v19, v19\n\t"
    "v_mfma_f32_32x32x16_bf16 v[208:223], a[124:127], v[60:63], v[208:223]\n\t"
    "ds_read_b128 a[28:31], v89 offset:32768\n\t"
    "ds_read_b64_tr_b16 a[94:95], v101 offset:55296\n\t"
    "v_cvt_pk_bf16_f32 v49, v18, v19\n\t"
    "v_add_f32 v80, v80, v19\n\t"
    "v_exp_f32 v20, v20\n\t"
    "s_waitcnt lgkmcnt(15)\n\t"
    "v_mfma_f32_32x32x16_bf16 v[32:47], a[32:35], v[128:131], 0\n\t"
    "s_add_u32 s76, s75, 114688\n\t"
    "s_add_u32 m0, s75, 114688\n\t"
    "ds_read_b64_tr_b16 a[96:97], v98 offset:57344\n\t"
    "v_add_f32 v80, v80, v20\n\t"
    "v_exp_f32 v21, v21\n\t"
    "v_mfma_f32_32x32x16_bf16 v[32:47], a[36:39], v[132:135], v[32:47]\n\t"
    "ds_read_b64_tr_b16 a[98:99], v98 offset:59392\n\t"
    "ds_read_b128 a[32:35], v82 offset:40960\n\t"
    "v_cvt_pk_bf16_f32 v50, v20, v21\n\t"
    "v_add_f32 v80, v80, v21\n\t"
    "v_exp_f32 v22, v22\n\t"
    "v_mfma_f32_32x32x16_bf16 v[32:47], a[40:43], v[136:139], v[32:47]\n\t"
    "buffer_load_dwordx4 v110, s[48:51], s76 offen nt lds\n\t"
    "ds_read_b64_tr_b16 a[100:101], v99 offset:57344\n\t"
    "v_add_f32 v80, v80, v22\n\t"
    "v_exp_f32 v23, v23\n\t"
    "v_mfma_f32_32x32x16_bf16 v[32:47], a[44:47], v[140:143], v[32:47]\n\t"
    "ds_read_b64_tr_b16 a[102:103], v99 offset:59392\n\t"
    "ds_read_b128 a[36:39], v83 offset:40960\n\t"
    "v_cvt_pk_bf16_f32 v51, v22, v23\n\t"
    "v_add_f32 v80, v80, v23\n\t"
    "v_exp_f32 v24, v24\n\t"
    "v_mfma_f32_32x32x16_bf16 v[32:47], a[48:51], v[144:147], v[32:47]\n\t"
    "ds_read_b64_tr_b16 a[104:105], v100 offset:57344\n\t"
    "v_add_f32 v80, v80, v24\n\t"
    "v_exp_f32 v25, v25\n\t"
    "v_mfma_f32_32x32x16_bf16 v[32:47], a[52:55], v[148:151], v[32:47]\n\t"
    "ds_read_b64_tr_b16 a[106:107], v100 offset:59392\n\t"
    "ds_read_b128 a[40:43], v84 offset:40960\n\t"
    "v_cvt_pk_bf16_f32 v52, v24, v25\n\t"
    "v_add_f32 v80, v80, v25\n\t"
    "v_exp_f32 v26, v26\n\t"
    "v_mfma_f32_32x32x16_bf16 v[32:47], a[56:59], v[152:155], v[32:47]\n\t"
    "buffer_load_dwordx4 v110, s[48:51], s76 offen offset:1024 nt lds\n\t"
    "ds_read_b64_tr_b16 a[108:109], v101 offset:57344\n\t"
    "v_add_f32 v80, v80, v26\n\t"
    "v_exp_f32 v27, v27\n\t"
    "v_mfma_f32_32x32x16_bf16 v[32:47], a[60:63], v[156:159], v[32:47]\n\t"
    "ds_read_b64_tr_b16 a[110:111], v101 offset:59392\n\t"
    "ds_read_b128 a[44:47], v85 offset:40960\n\t"
    "v_cvt_pk_bf16_f32 v53, v26, v27\n\t"
    "v_add_f32 v80, v80, v27\n\t"
    "v_exp_f32 v28, v28\n\t"
    "s_waitcnt lgkmcnt(12)\n\t"
    "v_mfma_f32_32x32x16_bf16 v[160:175], a[64:67], v[48:51], v[160:175]\n\t"
    "ds_read_b64_tr_b16 a[112:113], v98 offset:61440\n\t"
    "v_add_f32 v80, v80, v28\n\t"
    "v_exp_f32 v29, v29\n\t"
    "v_mfma_f32_32x32x16_bf16 v[176:191], a[68:71], v[48:51], v[176:191]\n\t"
    "ds_read_b64_tr_b16 a[114:115], v98 offset:63488\n\t"
    "ds_read_b128 a[48:51], v86 offset:40960\n\t"
    "v_cvt_pk_bf16_f32 v54, v28, v29\n\t"
    "v_add_f32 v80, v80, v29\n\t"
    "v_exp_f32 v30, v30\n\t"
    "v_mfma_f32_32x32x16_bf16 v[192:207], a[72:75], v[48:51], v[192:207]\n\t"
    "buffer_load_dwordx4 v110, s[48:51], s76 offen offset:2048 nt lds\n\t"
    "ds_read_b64_tr_b16 a[116:117], v99 offset:61440\n\t"
    "v_add_f32 v80, v80, v30\n\t"
    "v_exp_f32 v31, v31\n\t"
    "v_mfma_f32_32x32x16_bf16 v[208:223], a[76:79], v[48:51], v[208:223]\n\t"
    "ds_read_b64_tr_b16 a[118:119], v99 offset:63488\n\t"
    "ds_read_b128 a[52:55], v87 offset:40960\n\t"
    "v_cvt_pk_bf16_f32 v55, v30, v31\n\t"
    "v_add_f32 v80, v80, v31\n\t"
    "v_exp_f32 v32, v32\n\t"
    "v_mfma_f32_32x32x16_bf16 v[160:175], a[80:83], v[52:55], v[160:175]\n\t"
    "ds_read_b64_tr_b16 a[120:121], v100 offset:61440\n\t"
    "v_add_f32 v80, v80, v32\n\t"
    "v_exp_f32 v33, v33\n\t"
    "v_mfma_f32_32x32x16_bf16 v[176:191], a[84:87], v[52:55], v[176:191]\n\t"
    "ds_read_b64_tr_b16 a[122:123], v100 offset:63488\n\t"
    "ds_read_b128 a[56:59], v88 offset:40960\n\t"
    "v_cvt_pk_bf16_f32 v56, v32, v33\n\t"
    "v_add_f32 v80, v80, v33\n\t"
    "v_exp_f32 v34, v34\n\t"
    "v_mfma_f32_32x32x16_bf16 v[192:207], a[88:91], v[52:55], v[192:207]\n\t"
    "buffer_load_dwordx4 v110, s[48:51], s76 offen offset:3072 nt lds\n\t"
    "ds_read_b64_tr_b16 a[124:125], v101 offset:61440\n\t"
    "v_add_f32 v80, v80, v34\n\t"
    "v_exp_f32 v35, v35\n\t"
    "v_mfma_f32_32x32x16_bf16 v[208:223], a[92:95], v[52:55], v[208:223]\n\t"
    "ds_read_b64_tr_b16 a[126:127], v101 offset:63488\n\t"
    "ds_read_b128 a[60:63], v89 offset:40960\n\t"
    "v_cvt_pk_bf16_f32 v57, v34, v35\n\t"
    "v_add_f32 v80, v80, v35\n\t"
    "v_exp_f32 v36, v36\n\t"
    "s_waitcnt vmcnt(8)\n\t"
    "s_barrier\n\t"
    "v_mfma_f32_32x32x16_bf16 v[16:31], a[0:3], v[128:131], 0\n\t"
    "s_add_u32 s76, s75, 147456\n\t"
    "s_add_u32 m0, s75, 0\n\t"
    "ds_read_b64_tr_b16 a[64:65], v102 offset:16384\n\t"
    "v_add_f32 v80, v80, v36\n\t"
    "v_exp_f32 v37, v37\n\t"
    "v_mfma_f32_32x32x16_bf16 v[16:31], a[4:7], v[132:135], v[16:31]\n\t"
    "ds_read_b128 a[0:3], v90\n\t"
    "ds_read_b64_tr_b16 a[66:67], v102 offset:18432\n\t"
    "v_cvt_pk_bf16_f32 v58, v36, v37\n\t"
    "v_add_f32 v80, v80, v37\n\t"
    "v_exp_f32 v38, v38\n\t"
    "v_mfma_f32_32x32x16_bf16 v[16:31], a[8:11], v[136:139], v[16:31]\n\t"
    "buffer_load_dwordx4 v106, s[44:47], s76 offen nt lds\n\t"
    "ds_read_b64_tr_b16 a[68:69], v103 offset:16384\n\t"
    "v_add_f32 v80, v80, v38\n\t"
    "v_exp_f32 v39, v39\n\t"
    "v_mfma_f32_32x32x16_bf16 v[16:31], a[12:15], v[140:143], v[16:31]\n\t"
    "ds_read_b128 a[4:7], v91\n\t"
    "ds_read_b64_tr_b16 a[70:71], v103 offset:18432\n\t"
    "v_cvt_pk_bf16_f32 v59, v38, v39\n\t"
    "v_add_f32 v80, v80, v39\n\t"
    "v_exp_f32 v40, v40\n\t"
    "v_mfma_f32_32x32x16_bf16 v[16:31], a[16:19], v[144:147], v[16:31]\n\t"
    "ds_read_b64_tr_b16 a[72:73], v104 offset:16384\n\t"
    "v_add_f32 v80, v80, v40\n\t"
    "v_exp_f32 v41, v41\n\t"
    "v_mfma_f32_32x32x16_bf16 v[16:31], a[20:23], v[148:151], v[16:31]\n\t"
    "ds_read_b128 a[8:11], v92\n\t"
    "ds_read_b64_tr_b16 a[74:75], v104 offset:18432\n\t"
    "v_cvt_pk_bf16_f32 v60, v40, v41\n\t"
    "v_add_f32 v80, v80, v41\n\t"
    "v_exp_f32 v42, v42\n\t"
    "v_mfma_f32_32x32x16_bf16 v[16:31], a[24:27], v[152:155], v[16:31]\n\t"
    "buffer_load_dwordx4 v107, s[44:47], s76 offen offset:1024 nt lds\n\t"
    "ds_read_b64_tr_b16 a[76:77], v105 offset:16384\n\t"
    "v_add_f32 v80, v80, v42\n\t"
    "v_exp_f32 v43, v43\n\t"
    "v_mfma_f32_32x32x16_bf16 v[16:31], a[28:31], v[156:159], v[16:31]\n\t"
    "ds_read_b128 a[12:15], v93\n\t"
    "ds_read_b64_tr_b16 a[78:79], v105 offset:18432\n\t"
    "v_cvt_pk_bf16_f32 v61, v42, v43\n\t"
    "v_add_f32 v80, v80, v43\n\t"
    "v_exp_f32 v44, v44\n\t"
    "s_waitcnt lgkmcnt(13)\n\t"
    "v_mfma_f32_32x32x16_bf16 v[160:175], a[96:99], v[56:59], v[160:175]\n\t"
    "ds_read_b64_tr_b16 a[80:81], v102 offset:20480\n\t"
    "v_add_f32 v80, v80, v44\n\t"
    "v_exp_f32 v45, v45\n\t"
    "v_mfma_f32_32x32x16_bf16 v[176:191], a[100:103], v[56:59], v[176:191]\n\t"
    "ds_read_b128 a[16:19], v94\n\t"
    "ds_read_b64_tr_b16 a[82:83], v102 offset:22528\n\t"
    "v_cvt_pk_bf16_f32 v62, v44, v45\n\t"
    "v_add_f32 v80, v80, v45\n\t"
    "v_exp_f32 v46, v46\n\t"
    "v_mfma_f32_32x32x16_bf16 v[192:207], a[104:107], v[56:59], v[192:207]\n\t"
    "buffer_load_dwordx4 v108, s[44:47], s76 offen offset:2048 nt lds\n\t"
    "ds_read_b64_tr_b16 a[84:85], v103 offset:20480\n\t"
    "v_add_f32 v80, v80, v46\n\t"
    "v_exp_f32 v47, v47\n\t"
    "v_mfma_f32_32x32x16_bf16 v[208:223], a[108:111], v[56:59], v[208:223]\n\t"
    "ds_read_b128 a[20:23], v95\n\t"
    "ds_read_b64_tr_b16 a[86:87], v103 offset:22528\n\t"
    "v_cvt_pk_bf16_f32 v63, v46, v47\n\t"
    "v_add_f32 v80, v80, v47\n\t"
    "v_exp_f32 v16, v16\n\t"
    "v_mfma_f32_32x32x16_bf16 v[160:175], a[112:115], v[60:63], v[160:175]\n\t"
    "ds_read_b64_tr_b16 a[88:89], v104 offset:20480\n\t"
    "v_add_f32 v80, v80, v16\n\t"
    "v_exp_f32 v17, v17\n\t"
    "v_mfma_f32_32x32x16_bf16 v[176:191], a[116:119], v[60:63], v[176:191]\n\t"
    "ds_read_b128 a[24:27], v96\n\t"
    "ds_read_b64_tr_b16 a[90:91], v104 offset:22528\n\t"
    "v_cvt_pk_bf16_f32 v48, v16, v17\n\t"
    "v_add_f32 v80, v80, v17\n\t"
    "v_exp_f32 v18, v18\n\t"
    "v_mfma_f32_32x32x16_bf16 v[192:207], a[120:123], v[60:63], v[192:207]\n\t"
    "buffer_load_dwordx4 v109, s[44:47], s76 offen offset:3072 nt lds\n\t"
    "ds_read_b64_tr_b16 a[92:93], v105 offset:20480\n\t"
    "v_add_f32 v80, v80, v18\n\t"
    "v_exp_f32 v19, v19\n\t"
    "v_mfma_f32_32x32x16_bf16 v[208:223], a[124:127], v[60:63], v[208:223]\n\t"
    "ds_read_b128 a[28:31], v97\n\t"
    "ds_read_b64_tr_b16 a[94:95], v105 offset:22528\n\t"
    "v_cvt_pk_bf16_f32 v49, v18, v19\n\t"
    "v_add_f32 v80, v80, v19\n\t"
    "v_exp_f32 v20, v20\n\t"
    "s_waitcnt lgkmcnt(15)\n\t"
    "v_mfma_f32_32x32x16_bf16 v[32:47], a[32:35], v[128:131], 0\n\t"
    "s_add_u32 s76, s75, 131072\n\t"
    "s_add_u32 m0, s75, 16384\n\t"
    "ds_read_b64_tr_b16 a[96:97], v102 offset:24576\n\t"
    "v_add_f32 v80, v80, v20\n\t"
    "v_exp_f32 v21, v21\n\t"
    "v_mfma_f32_32x32x16_bf16 v[32:47], a[36:39], v[132:135], v[32:47]\n\t"
    "ds_read_b64_tr_b16 a[98:99], v102 offset:26624\n\t"
    "ds_read_b128 a[32:35], v90 offset:8192\n\t"
    "v_cvt_pk_bf16_f32 v50, v20, v21\n\t"
    "v_add_f32 v80, v80, v21\n\t"
    "v_exp_f32 v22, v22\n\t"
    "v_mfma_f32_32x32x16_bf16 v[32:47], a[40:43], v[136:139], v[32:47]\n\t"
    "buffer_load_dwordx4 v110, s[48:51], s76 offen nt lds\n\t"
    "ds_read_b64_tr_b16 a[100:101], v103 offset:24576\n\t"
    "v_add_f32 v80, v80, v22\n\t"
    "v_exp_f32 v23, v23\n\t"
    "v_mfma_f32_32x32x16_bf16 v[32:47], a[44:47], v[140:143], v[32:47]\n\t"
    "ds_read_b64_tr_b16 a[102:103], v103 offset:26624\n\t"
    "ds_read_b128 a[36:39], v91 offset:8192\n\t"
    "v_cvt_pk_bf16_f32 v51, v22, v23\n\t"
    "v_add_f32 v80, v80, v23\n\t"
    "v_exp_f32 v24, v24\n\t"
    "v_mfma_f32_32x32x16_bf16 v[32:47], a[48:51], v[144:147], v[32:47]\n\t"
    "ds_read_b64_tr_b16 a[104:105], v104 offset:24576\n\t"
    "v_add_f32 v80, v80, v24\n\t"
    "v_exp_f32 v25, v25\n\t"
    "v_mfma_f32_32x32x16_bf16 v[32:47], a[52:55], v[148:151], v[32:47]\n\t"
    "ds_read_b64_tr_b16 a[106:107], v104 offset:26624\n\t"
    "ds_read_b128 a[40:43], v92 offset:8192\n\t"
    "v_cvt_pk_bf16_f32 v52, v24, v25\n\t"
    "v_add_f32 v80, v80, v25\n\t"
    "v_exp_f32 v26, v26\n\t"
    "v_mfma_f32_32x32x16_bf16 v[32:47], a[56:59], v[152:155], v[32:47]\n\t"
    "buffer_load_dwordx4 v110, s[48:51], s76 offen offset:1024 nt lds\n\t"
    "ds_read_b64_tr_b16 a[108:109], v105 offset:24576\n\t"
    "v_add_f32 v80, v80, v26\n\t"
    "v_exp_f32 v27, v27\n\t"
    "v_mfma_f32_32x32x16_bf16 v[32:47], a[60:63], v[156:159], v[32:47]\n\t"
    "ds_read_b64_tr_b16 a[110:111], v105 offset:26624\n\t"
    "ds_read_b128 a[44:47], v93 offset:8192\n\t"
    "v_cvt_pk_bf16_f32 v53, v26, v27\n\t"
    "v_add_f32 v80, v80, v27\n\t"
    "v_exp_f32 v28, v28\n\t"
    "s_waitcnt lgkmcnt(12)\n\t"
    "v_mfma_f32_32x32x16_bf16 v[160:175], a[64:67], v[48:51], v[160:175]\n\t"
    "ds_read_b64_tr_b16 a[112:113], v102 offset:28672\n\t"
    "v_add_f32 v80, v80, v28\n\t"
    "v_exp_f32 v29, v29\n\t"
    "v_mfma_f32_32x32x16_bf16 v[176:191], a[68:71], v[48:51], v[176:191]\n\t"
    "ds_read_b64_tr_b16 a[114:115], v102 offset:30720\n\t"
    "ds_read_b128 a[48:51], v94 offset:8192\n\t"
    "v_cvt_pk_bf16_f32 v54, v28, v29\n\t"
    "v_add_f32 v80, v80, v29\n\t"
    "v_exp_f32 v30, v30\n\t"
    "v_mfma_f32_32x32x16_bf16 v[192:207], a[72:75], v[48:51], v[192:207]\n\t"
    "buffer_load_dwordx4 v110, s[48:51], s76 offen offset:2048 nt lds\n\t"
    "ds_read_b64_tr_b16 a[116:117], v103 offset:28672\n\t"
    "v_add_f32 v80, v80, v30\n\t"
    "v_exp_f32 v31, v31\n\t"
    "v_mfma_f32_32x32x16_bf16 v[208:223], a[76:79], v[48:51], v[208:223]\n\t"
    "ds_read_b64_tr_b16 a[118:119], v103 offset:30720\n\t"
    "ds_read_b128 a[52:55], v95 offset:8192\n\t"
    "v_cvt_pk_bf16_f32 v55, v30, v31\n\t"
    "v_add_f32 v80, v80, v31\n\t"
    "v_exp_f32 v32, v32\n\t"
    "v_mfma_f32_32x32x16_bf16 v[160:175], a[80:83], v[52:55], v[160:175]\n\t"
    "ds_read_b64_tr_b16 a[120:121], v104 offset:28672\n\t"
    "v_add_f32 v80, v80, v32\n\t"
    "v_exp_f32 v33, v33\n\t"
    "v_mfma_f32_32x32x16_bf16 v[176:191], a[84:87], v[52:55], v[176:191]\n\t"
    "ds_read_b64_tr_b16 a[122:123], v104 offset:30720\n\t"
    "ds_read_b128 a[56:59], v96 offset:8192\n\t"
    "v_cvt_pk_bf16_f32 v56, v32, v33\n\t"
    "v_add_f32 v80, v80, v33\n\t"
    "v_exp_f32 v34, v34\n\t"
    "v_mfma_f32_32x32x16_bf16 v[192:207], a[88:91], v[52:55], v[192:207]\n\t"
    "buffer_load_dwordx4 v110, s[48:51], s76 offen offset:3072 nt lds\n\t"
    "ds_read_b64_tr_b16 a[124:125], v105 offset:28672\n\t"
    "v_add_f32 v80, v80, v34\n\t"
    "v_exp_f32 v35, v35\n\t"
    "v_mfma_f32_32x32x16_bf16 v[208:223], a[92:95], v[52:55], v[208:223]\n\t"
    "ds_read_b64_tr_b16 a[126:127], v105 offset:30720\n\t"
    "ds_read_b128 a[60:63], v97 offset:8192\n\t"
    "v_cvt_pk_bf16_f32 v57, v34, v35\n\t"
    "v_add_f32 v80, v80, v35\n\t"
    "v_exp_f32 v36, v36\n\t"
    "s_waitcnt vmcnt(8)\n\t"
    "s_barrier\n\t"
    "v_mfma_f32_32x32x16_bf16 v[16:31], a[0:3], v[128:131], 0\n\t"
    "s_add_u32 s76, s75, 163840\n\t"
    "s_add_u32 m0, s75, 32768\n\t"
    "ds_read_b64_tr_b16 a[64:65], v102 offset:49152\n\t"
    "v_add_f32 v80, v80, v36\n\t"
    "v_exp_f32 v37, v37\n\t"
    "v_mfma_f32_32x32x16_bf16 v[16:31], a[4:7], v[132:135], v[16:31]\n\t"
    "ds_read_b128 a[0:3], v90 offset:32768\n\t"
    "ds_read_b64_tr_b16 a[66:67], v102 offset:51200\n\t"
    "v_cvt_pk_bf16_f32 v58, v36, v37\n\t"
    "v_add_f32 v80, v80, v37\n\t"
    "v_exp_f32 v38, v38\n\t"
    "v_mfma_f32_32x32x16_bf16 v[16:31], a[8:11], v[136:139], v[16:31]\n\t"
    "buffer_load_dwordx4 v106, s[44:47], s76 offen nt lds\n\t"
    "ds_read_b64_tr_b16 a[68:69], v103 offset:49152\n\t"
    "v_add_f32 v80, v80, v38\n\t"
    "v_exp_f32 v39, v39\n\t"
    "v_mfma_f32_32x32x16_bf16 v[16:31], a[12:15], v[140:143], v[16:31]\n\t"
    "ds_read_b128 a[4:7], v91 offset:32768\n\t"
    "ds_read_b64_tr_b16 a[70:71], v103 offset:51200\n\t"
    "v_cvt_pk_bf16_f32 v59, v38, v39\n\t"
    "v_add_f32 v80, v80, v39\n\t"
    "v_exp_f32 v40, v40\n\t"
    "v_mfma_f32_32x32x16_bf16 v[16:31], a[16:19], v[144:147], v[16:31]\n\t"
    "ds_read_b64_tr_b16 a[72:73], v104 offset:49152\n\t"
    "v_add_f32 v80, v80, v40\n\t"
    "v_exp_f32 v41, v41\n\t"
    "v_mfma_f32_32x32x16_bf16 v[16:31], a[20:23], v[148:151], v[16:31]\n\t"
    "ds_read_b128 a[8:11], v92 offset:32768\n\t"
    "ds_read_b64_tr_b16 a[74:75], v104 offset:51200\n\t"
    "v_cvt_pk_bf16_f32 v60, v40, v41\n\t"
    "v_add_f32 v80, v80, v41\n\t"
    "v_exp_f32 v42, v42\n\t"
    "v_mfma_f32_32x32x16_bf16 v[16:31], a[24:27], v[152:155], v[16:31]\n\t"
    "buffer_load_dwordx4 v107, s[44:47], s76 offen offset:1024 nt lds\n\t"
    "ds_read_b64_tr_b16 a[76:77], v105 offset:49152\n\t"
    "v_add_f32 v80, v80, v42\n\t"
    "v_exp_f32 v43, v43\n\t"
    "v_mfma_f32_32x32x16_bf16 v[16:31], a[28:31], v[156:159], v[16:31]\n\t"
    "ds_read_b128 a[12:15], v93 offset:32768\n\t"
    "ds_read_b64_tr_b16 a[78:79], v105 offset:51200\n\t"
    "v_cvt_pk_bf16_f32 v61, v42, v43\n\t"
    "v_add_f32 v80, v80, v43\n\t"
    "v_exp_f32 v44, v44\n\t"
    "s_waitcnt lgkmcnt(13)\n\t"
    "v_mfma_f32_32x32x16_bf16 v[160:175], a[96:99], v[56:59], v[160:175]\n\t"
    "ds_read_b64_tr_b16 a[80:81], v102 offset:53248\n\t"
    "v_add_f32 v80, v80, v44\n\t"
    "v_exp_f32 v45, v45\n\t"
    "v_mfma_f32_32x32x16_bf16 v[176:191], a[100:103], v[56:59], v[176:191]\n\t"
    "ds_read_b128 a[16:19], v94 offset:32768\n\t"
    "ds_read_b64_tr_b16 a[82:83], v102 offset:55296\n\t"
    "v_cvt_pk_bf16_f32 v62, v44, v45\n\t"
    "v_add_f32 v80, v80, v45\n\t"
    "v_exp_f32 v46, v46\n\t"
    "v_mfma_f32_32x32x16_bf16 v[192:207], a[104:107], v[56:59], v[192:207]\n\t"
    "buffer_load_dwordx4 v108, s[44:47], s76 offen offset:2048 nt lds\n\t"
    "ds_read_b64_tr_b16 a[84:85], v103 offset:53248\n\t"
    "v_add_f32 v80, v80, v46\n\t"
    "v_exp_f32 v47, v47\n\t"
    "v_mfma_f32_32x32x16_bf16 v[208:223], a[108:111], v[56:59], v[208:223]\n\t"
    "ds_read_b128 a[20:23], v95 offset:32768\n\t"
    "ds_read_b64_tr_b16 a[86:87], v103 offset:55296\n\t"
    "v_cvt_pk_bf16_f32 v63, v46, v47\n\t"
    "v_add_f32 v80, v80, v47\n\t"
    "v_exp_f32 v16, v16\n\t"
    "v_mfma_f32_32x32x16_bf16 v[160:175], a[112:115], v[60:63], v[160:175]\n\t"
    "ds_read_b64_tr_b16 a[88:89], v104 offset:53248\n\t"
    "v_add_f32 v80, v80, v16\n\t"
    "v_exp_f32 v17, v17\n\t"
    "v_mfma_f32_32x32x16_bf16 v[176:191], a[116:119], v[60:63], v[176:191]\n\t"
    "ds_read_b128 a[24:27], v96 offset:32768\n\t"
    "ds_read_b64_tr_b16 a[90:91], v104 offset:55296\n\t"
    "v_cvt_pk_bf16_f32 v48, v16, v17\n\t"
    "v_add_f32 v80, v80, v17\n\t"
    "v_exp_f32 v18, v18\n\t"
    "v_mfma_f32_32x32x16_bf16 v[192:207], a[120:123], v[60:63], v[192:207]\n\t"
    "buffer_load_dwordx4 v109, s[44:47], s76 offen offset:3072 nt lds\n\t"
    "ds_read_b64_tr_b16 a[92:93], v105 offset:53248\n\t"
    "v_add_f32 v80, v80, v18\n\t"
    "v_exp_f32 v19, v19\n\t"
    "v_mfma_f32_32x32x16_bf16 v[208:223], a[124:127], v[60:63], v[208:223]\n\t"
    "ds_read_b128 a[28:31], v97 offset:32768\n\t"
    "ds_read_b64_tr_b16 a[94:95], v105 offset:55296\n\t"
    "v_cvt_pk_bf16_f32 v49, v18, v19\n\t"
    "v_add_f32 v80, v80, v19\n\t"
    "v_exp_f32 v20, v20\n\t"
    "s_waitcnt lgkmcnt(15)\n\t"
    "v_mfma_f32_32x32x16_bf16 v[32:47], a[32:35], v[128:131], 0\n\t"
    "s_add_u32 s76, s75, 147456\n\t"
    "s_add_u32 m0, s75, 49152\n\t"
    "ds_read_b64_tr_b16 a[96:97], v102 offset:57344\n\t"
    "v_add_f32 v80, v80, v20\n\t"
    "v_exp_f32 v21, v21\n\t"
    "v_mfma_f32_32x32x16_bf16 v[32:47], a[36:39], v[132:135], v[32:47]\n\t"
    "ds_read_b64_tr_b16 a[98:99], v102 offset:59392\n\t"
    "ds_read_b128 a[32:35], v90 offset:40960\n\t"
    "v_cvt_pk_bf16_f32 v50, v20, v21\n\t"
    "v_add_f32 v80, v80, v21\n\t"
    "v_exp_f32 v22, v22\n\t"
    "v_mfma_f32_32x32x16_bf16 v[32:47], a[40:43], v[136:139], v[32:47]\n\t"
    "buffer_load_dwordx4 v110, s[48:51], s76 offen nt lds\n\t"
    "ds_read_b64_tr_b16 a[100:101], v103 offset:57344\n\t"
    "v_add_f32 v80, v80, v22\n\t"
    "v_exp_f32 v23, v23\n\t"
    "v_mfma_f32_32x32x16_bf16 v[32:47], a[44:47], v[140:143], v[32:47]\n\t"
    "ds_read_b64_tr_b16 a[102:103], v103 offset:59392\n\t"
    "ds_read_b128 a[36:39], v91 offset:40960\n\t"
    "v_cvt_pk_bf16_f32 v51, v22, v23\n\t"
    "v_add_f32 v80, v80, v23\n\t"
    "v_exp_f32 v24, v24\n\t"
    "v_mfma_f32_32x32x16_bf16 v[32:47], a[48:51], v[144:147], v[32:47]\n\t"
    "ds_read_b64_tr_b16 a[104:105], v104 offset:57344\n\t"
    "v_add_f32 v80, v80, v24\n\t"
    "v_exp_f32 v25, v25\n\t"
    "v_mfma_f32_32x32x16_bf16 v[32:47], a[52:55], v[148:151], v[32:47]\n\t"
    "ds_read_b64_tr_b16 a[106:107], v104 offset:59392\n\t"
    "ds_read_b128 a[40:43], v92 offset:40960\n\t"
    "v_cvt_pk_bf16_f32 v52, v24, v25\n\t"
    "v_add_f32 v80, v80, v25\n\t"
    "v_exp_f32 v26, v26\n\t"
    "v_mfma_f32_32x32x16_bf16 v[32:47], a[56:59], v[152:155], v[32:47]\n\t"
    "buffer_load_dwordx4 v110, s[48:51], s76 offen offset:1024 nt lds\n\t"
    "ds_read_b64_tr_b16 a[108:109], v105 offset:57344\n\t"
    "v_add_f32 v80, v80, v26\n\t"
    "v_exp_f32 v27, v27\n\t"
    "v_mfma_f32_32x32x16_bf16 v[32:47], a[60:63], v[156:159], v[32:47]\n\t"
    "ds_read_b64_tr_b16 a[110:111], v105 offset:59392\n\t"
    "ds_read_b128 a[44:47], v93 offset:40960\n\t"
    "v_cvt_pk_bf16_f32 v53, v26, v27\n\t"
    "v_add_f32 v80, v80, v27\n\t"
    "v_exp_f32 v28, v28\n\t"
    "s_waitcnt lgkmcnt(12)\n\t"
    "v_mfma_f32_32x32x16_bf16 v[160:175], a[64:67], v[48:51], v[160:175]\n\t"
    "ds_read_b64_tr_b16 a[112:113], v102 offset:61440\n\t"
    "v_add_f32 v80, v80, v28\n\t"
    "v_exp_f32 v29, v29\n\t"
    "v_mfma_f32_32x32x16_bf16 v[176:191], a[68:71], v[48:51], v[176:191]\n\t"
    "ds_read_b64_tr_b16 a[114:115], v102 offset:63488\n\t"
    "ds_read_b128 a[48:51], v94 offset:40960\n\t"
    "v_cvt_pk_bf16_f32 v54, v28, v29\n\t"
    "v_add_f32 v80, v80, v29\n\t"
    "v_exp_f32 v30, v30\n\t"
    "v_mfma_f32_32x32x16_bf16 v[192:207], a[72:75], v[48:51], v[192:207]\n\t"
    "buffer_load_dwordx4 v110, s[48:51], s76 offen offset:2048 nt lds\n\t"
    "ds_read_b64_tr_b16 a[116:117], v103 offset:61440\n\t"
    "v_add_f32 v80, v80, v30\n\t"
    "v_exp_f32 v31, v31\n\t"
    "v_mfma_f32_32x32x16_bf16 v[208:223], a[76:79], v[48:51], v[208:223]\n\t"
    "ds_read_b64_tr_b16 a[118:119], v103 offset:63488\n\t"
    "ds_read_b128 a[52:55], v95 offset:40960\n\t"
    "v_cvt_pk_bf16_f32 v55, v30, v31\n\t"
    "v_add_f32 v80, v80, v31\n\t"
    "v_exp_f32 v32, v32\n\t"
    "v_mfma_f32_32x32x16_bf16 v[160:175], a[80:83], v[52:55], v[160:175]\n\t"
    "ds_read_b64_tr_b16 a[120:121], v104 offset:61440\n\t"
    "v_add_f32 v80, v80, v32\n\t"
    "v_exp_f32 v33, v33\n\t"
    "v_mfma_f32_32x32x16_bf16 v[176:191], a[84:87], v[52:55], v[176:191]\n\t"
    "ds_read_b64_tr_b16 a[122:123], v104 offset:63488\n\t"
    "ds_read_b128 a[56:59], v96 offset:40960\n\t"
    "v_cvt_pk_bf16_f32 v56, v32, v33\n\t"
    "v_add_f32 v80, v80, v33\n\t"
    "v_exp_f32 v34, v34\n\t"
    "v_mfma_f32_32x32x16_bf16 v[192:207], a[88:91], v[52:55], v[192:207]\n\t"
    "buffer_load_dwordx4 v110, s[48:51], s76 offen offset:3072 nt lds\n\t"
    "ds_read_b64_tr_b16 a[124:125], v105 offset:61440\n\t"
    "v_add_f32 v80, v80, v34\n\t"
    "v_exp_f32 v35, v35\n\t"
    "v_mfma_f32_32x32x16_bf16 v[208:223], a[92:95], v[52:55], v[208:223]\n\t"
    "ds_read_b64_tr_b16 a[126:127], v105 offset:63488\n\t"
    "ds_read_b128 a[60:63], v97 offset:40960\n\t"
    "v_cvt_pk_bf16_f32 v57, v34, v35\n\t"
    "v_add_f32 v80, v80, v35\n\t"
    "v_exp_f32 v36, v36\n\t"
    "s_waitcnt vmcnt(8)\n\t"
    "s_barrier\n\t"
    "v_mfma_f32_32x32x16_bf16 v[16:31], a[0:3], v[128:131], 0\n\t"
    "s_add_u32 s76, s75, 180224\n\t"
    "s_add_u32 m0, s75, 65536\n\t"
    "ds_read_b64_tr_b16 a[64:65], v98 offset:16384\n\t"
    "v_add_f32 v80, v80, v36\n\t"
    "v_exp_f32 v37, v37\n\t"
    "v_mfma_f32_32x32x16_bf16 v[16:31], a[4:7], v[132:135], v[16:31]\n\t"
    "ds_read_b128 a[0:3], v82\n\t"
    "ds_read_b64_tr_b16 a[66:67], v98 offset:18432\n\t"
    "v_cvt_pk_bf16_f32 v58, v36, v37\n\t"
    "v_add_f32 v80, v80, v37\n\t"
    "v_exp_f32 v38, v38\n\t"
    "v_mfma_f32_32x32x16_bf16 v[16:31], a[8:11], v[136:139], v[16:31]\n\t"
    "buffer_load_dwordx4 v106, s[44:47], s76 offen nt lds\n\t"
    "ds_read_b64_tr_b16 a[68:69], v99 offset:16384\n\t"
    "v_add_f32 v80, v80, v38\n\t"
    "v_exp_f32 v39, v39\n\t"
    "v_mfma_f32_32x32x16_bf16 v[16:31], a[12:15], v[140:143], v[16:31]\n\t"
    "ds_read_b128 a[4:7], v83\n\t"
    "ds_read_b64_tr_b16 a[70:71], v99 offset:18432\n\t"
    "v_cvt_pk_bf16_f32 v59, v38, v39\n\t"
    "v_add_f32 v80, v80, v39\n\t"
    "v_exp_f32 v40, v40\n\t"
    "v_mfma_f32_32x32x16_bf16 v[16:31], a[16:19], v[144:147], v[16:31]\n\t"
    "ds_read_b64_tr_b16 a[72:73], v100 offset:16384\n\t"
    "v_add_f32 v80, v80, v40\n\t"
    "v_exp_f32 v41, v41\n\t"
    "v_mfma_f32_32x32x16_bf16 v[16:31], a[20:23], v[148:151], v[16:31]\n\t"
    "ds_read_b128 a[8:11], v84\n\t"
    "ds_read_b64_tr_b16 a[74:75], v100 offset:18432\n\t"
    "v_cvt_pk_bf16_f32 v60, v40, v41\n\t"
    "v_add_f32 v80, v80, v41\n\t"
    "v_exp_f32 v42, v42\n\t"
    "v_mfma_f32_32x32x16_bf16 v[16:31], a[24:27], v[152:155], v[16:31]\n\t"
    "buffer_load_dwordx4 v107, s[44:47], s76 offen offset:1024 nt lds\n\t"
    "ds_read_b64_tr_b16 a[76:77], v101 offset:16384\n\t"
    "v_add_f32 v80, v80, v42\n\t"
    "v_exp_f32 v43, v43\n\t"
    "v_mfma_f32_32x32x16_bf16 v[16:31], a[28:31], v[156:159], v[16:31]\n\t"
    "ds_read_b128 a[12:15], v85\n\t"
    "ds_read_b64_tr_b16 a[78:79], v101 offset:18432\n\t"
    "v_cvt_pk_bf16_f32 v61, v42, v43\n\t"
    "v_add_f32 v80, v80, v43\n\t"
    "v_exp_f32 v44, v44\n\t"
    "s_waitcnt lgkmcnt(13)\n\t"
    "v_mfma_f32_32x32x16_bf16 v[160:175], a[96:99], v[56:59], v[160:175]\n\t"
    "ds_read_b64_tr_b16 a[80:81], v98 offset:20480\n\t"
    "v_add_f32 v80, v80, v44\n\t"
    "v_exp_f32 v45, v45\n\t"
    "v_mfma_f32_32x32x16_bf16 v[176:191], a[100:103], v[56:59], v[176:191]\n\t"
    "ds_read_b128 a[16:19], v86\n\t"
    "ds_read_b64_tr_b16 a[82:83], v98 offset:22528\n\t"
    "v_cvt_pk_bf16_f32 v62, v44, v45\n\t"
    "v_add_f32 v80, v80, v45\n\t"
    "v_exp_f32 v46, v46\n\t"
    "v_mfma_f32_32x32x16_bf16 v[192:207], a[104:107], v[56:59], v[192:207]\n\t"
    "buffer_load_dwordx4 v108, s[44:47], s76 offen offset:2048 nt lds\n\t"
    "ds_read_b64_tr_b16 a[84:85], v99 offset:20480\n\t"
    "v_add_f32 v80, v80, v46\n\t"
    "v_exp_f32 v47, v47\n\t"
    "v_mfma_f32_32x32x16_bf16 v[208:223], a[108:111], v[56:59], v[208:223]\n\t"
    "ds_read_b128 a[20:23], v87\n\t"
    "ds_read_b64_tr_b16 a[86:87], v99 offset:22528\n\t"
    "v_cvt_pk_bf16_f32 v63, v46, v47\n\t"
    "v_add_f32 v80, v80, v47\n\t"
    "v_exp_f32 v16, v16\n\t"
    "v_mfma_f32_32x32x16_bf16 v[160:175], a[112:115], v[60:63], v[160:175]\n\t"
    "ds_read_b64_tr_b16 a[88:89], v100 offset:20480\n\t"
    "v_add_f32 v80, v80, v16\n\t"
    "v_exp_f32 v17, v17\n\t"
    "v_mfma_f32_32x32x16_bf16 v[176:191], a[116:119], v[60:63], v[176:191]\n\t"
    "ds_read_b128 a[24:27], v88\n\t"
    "ds_read_b64_tr_b16 a[90:91], v100 offset:22528\n\t"
    "v_cvt_pk_bf16_f32 v48, v16, v17\n\t"
    "v_add_f32 v80, v80, v17\n\t"
    "v_exp_f32 v18, v18\n\t"
    "v_mfma_f32_32x32x16_bf16 v[192:207], a[120:123], v[60:63], v[192:207]\n\t"
    "buffer_load_dwordx4 v109, s[44:47], s76 offen offset:3072 nt lds\n\t"
    "ds_read_b64_tr_b16 a[92:93], v101 offset:20480\n\t"
    "v_add_f32 v80, v80, v18\n\t"
    "v_exp_f32 v19, v19\n\t"
    "v_mfma_f32_32x32x16_bf16 v[208:223], a[124:127], v[60:63], v[208:223]\n\t"
    "ds_read_b128 a[28:31], v89\n\t"
    "ds_read_b64_tr_b16 a[94:95], v101 offset:22528\n\t"
    "v_cvt_pk_bf16_f32 v49, v18, v19\n\t"
    "v_add_f32 v80, v80, v19\n\t"
    "v_exp_f32 v20, v20\n\t"
    "s_waitcnt lgkmcnt(15)\n\t"
    "v_mfma_f32_32x32x16_bf16 v[32:47], a[32:35], v[128:131], 0\n\t"
    "s_add_u32 s76, s75, 163840\n\t"
    "s_add_u32 m0, s75, 81920\n\t"
    "ds_read_b64_tr_b16 a[96:97], v98 offset:24576\n\t"
    "v_add_f32 v80, v80, v20\n\t"
    "v_exp_f32 v21, v21\n\t"
    "v_mfma_f32_32x32x16_bf16 v[32:47], a[36:39], v[132:135], v[32:47]\n\t"
    "ds_read_b64_tr_b16 a[98:99], v98 offset:26624\n\t"
    "ds_read_b128 a[32:35], v82 offset:8192\n\t"
    "v_cvt_pk_bf16_f32 v50, v20, v21\n\t"
    "v_add_f32 v80, v80, v21\n\t"
    "v_exp_f32 v22, v22\n\t"
    "v_mfma_f32_32x32x16_bf16 v[32:47], a[40:43], v[136:139], v[32:47]\n\t"
    "buffer_load_dwordx4 v110, s[48:51], s76 offen nt lds\n\t"
    "ds_read_b64_tr_b16 a[100:101], v99 offset:24576\n\t"
    "v_add_f32 v80, v80, v22\n\t"
    "v_exp_f32 v23, v23\n\t"
    "v_mfma_f32_32x32x16_bf16 v[32:47], a[44:47], v[140:143], v[32:47]\n\t"
    "ds_read_b64_tr_b16 a[102:103], v99 offset:26624\n\t"
    "ds_read_b128 a[36:39], v83 offset:8192\n\t"
    "v_cvt_pk_bf16_f32 v51, v22, v23\n\t"
    "v_add_f32 v80, v80, v23\n\t"
    "v_exp_f32 v24, v24\n\t"
    "v_mfma_f32_32x32x16_bf16 v[32:47], a[48:51], v[144:147], v[32:47]\n\t"
    "ds_read_b64_tr_b16 a[104:105], v100 offset:24576\n\t"
    "v_add_f32 v80, v80, v24\n\t"
    "v_exp_f32 v25, v25\n\t"
    "v_mfma_f32_32x32x16_bf16 v[32:47], a[52:55], v[148:151], v[32:47]\n\t"
    "ds_read_b64_tr_b16 a[106:107], v100 offset:26624\n\t"
    "ds_read_b128 a[40:43], v84 offset:8192\n\t"
    "v_cvt_pk_bf16_f32 v52, v24, v25\n\t"
    "v_add_f32 v80, v80, v25\n\t"
    "v_exp_f32 v26, v26\n\t"
    "v_mfma_f32_32x32x16_bf16 v[32:47], a[56:59], v[152:155], v[32:47]\n\t"
    "buffer_load_dwordx4 v110, s[48:51], s76 offen offset:1024 nt lds\n\t"
    "ds_read_b64_tr_b16 a[108:109], v101 offset:24576\n\t"
    "v_add_f32 v80, v80, v26\n\t"
    "v_exp_f32 v27, v27\n\t"
    "v_mfma_f32_32x32x16_bf16 v[32:47], a[60:63], v[156:159], v[32:47]\n\t"
    "ds_read_b64_tr_b16 a[110:111], v101 offset:26624\n\t"
    "ds_read_b128 a[44:47], v85 offset:8192\n\t"
    "v_cvt_pk_bf16_f32 v53, v26, v27\n\t"
    "v_add_f32 v80, v80, v27\n\t"
    "v_exp_f32 v28, v28\n\t"
    "s_waitcnt lgkmcnt(12)\n\t"
    "v_mfma_f32_32x32x16_bf16 v[160:175], a[64:67], v[48:51], v[160:175]\n\t"
    "ds_read_b64_tr_b16 a[112:113], v98 offset:28672\n\t"
    "v_add_f32 v80, v80, v28\n\t"
    "v_exp_f32 v29, v29\n\t"
    "v_mfma_f32_32x32x16_bf16 v[176:191], a[68:71], v[48:51], v[176:191]\n\t"
    "ds_read_b64_tr_b16 a[114:115], v98 offset:30720\n\t"
    "ds_read_b128 a[48:51], v86 offset:8192\n\t"
    "v_cvt_pk_bf16_f32 v54, v28, v29\n\t"
    "v_add_f32 v80, v80, v29\n\t"
    "v_exp_f32 v30, v30\n\t"
    "v_mfma_f32_32x32x16_bf16 v[192:207], a[72:75], v[48:51], v[192:207]\n\t"
    "buffer_load_dwordx4 v110, s[48:51], s76 offen offset:2048 nt lds\n\t"
    "ds_read_b64_tr_b16 a[116:117], v99 offset:28672\n\t"
    "v_add_f32 v80, v80, v30\n\t"
    "v_exp_f32 v31, v31\n\t"
    "v_mfma_f32_32x32x16_bf16 v[208:223], a[76:79], v[48:51], v[208:223]\n\t"
    "ds_read_b64_tr_b16 a[118:119], v99 offset:30720\n\t"
    "ds_read_b128 a[52:55], v87 offset:8192\n\t"
    "v_cvt_pk_bf16_f32 v55, v30, v31\n\t"
    "v_add_f32 v80, v80, v31\n\t"
    "v_exp_f32 v32, v32\n\t"
    "v_mfma_f32_32x32x16_bf16 v[160:175], a[80:83], v[52:55], v[160:175]\n\t"
    "ds_read_b64_tr_b16 a[120:121], v100 offset:28672\n\t"
    "v_add_f32 v80, v80, v32\n\t"
    "v_exp_f32 v33, v33\n\t"
    "v_mfma_f32_32x32x16_bf16 v[176:191], a[84:87], v[52:55], v[176:191]\n\t"
    "ds_read_b64_tr_b16 a[122:123], v100 offset:30720\n\t"
    "ds_read_b128 a[56:59], v88 offset:8192\n\t"
    "v_cvt_pk_bf16_f32 v56, v32, v33\n\t"
    "v_add_f32 v80, v80, v33\n\t"
    "v_exp_f32 v34, v34\n\t"
    "v_mfma_f32_32x32x16_bf16 v[192:207], a[88:91], v[52:55], v[192:207]\n\t"
    "buffer_load_dwordx4 v110, s[48:51], s76 offen offset:3072 nt lds\n\t"
    "ds_read_b64_tr_b16 a[124:125], v101 offset:28672\n\t"
    "v_add_f32 v80, v80, v34\n\t"
    "v_exp_f32 v35, v35\n\t"
    "v_mfma_f32_32x32x16_bf16 v[208:223], a[92:95], v[52:55], v[208:223]\n\t"
    "ds_read_b64_tr_b16 a[126:127], v101 offset:30720\n\t"
    "ds_read_b128 a[60:63], v89 offset:8192\n\t"
    "v_cvt_pk_bf16_f32 v57, v34, v35\n\t"
    "v_add_f32 v80, v80, v35\n\t"
    "v_exp_f32 v36, v36\n\t"
    "s_waitcnt vmcnt(8)\n\t"
    "s_barrier\n\t"
    "v_mfma_f32_32x32x16_bf16 v[16:31], a[0:3], v[128:131], 0\n\t"
    "s_add_u32 s76, s75, 196608\n\t"
    "s_add_u32 m0, s75, 98304\n\t"
    "ds_read_b64_tr_b16 a[64:65], v98 offset:49152\n\t"
    "v_add_f32 v80, v80, v36\n\t"
    "v_exp_f32 v37, v37\n\t"
    "v_mfma_f32_32x32x16_bf16 v[16:31], a[4:7], v[132:135], v[16:31]\n\t"
    "ds_read_b128 a[0:3], v82 offset:32768\n\t"
    "ds_read_b64_tr_b16 a[66:67], v98 offset:51200\n\t"
    "v_cvt_pk_bf16_f32 v58, v36, v37\n\t"
    "v_add_f32 v80, v80, v37\n\t"
    "v_exp_f32 v38, v38\n\t"
    "v_mfma_f32_32x32x16_bf16 v[16:31], a[8:11], v[136:139], v[16:31]\n\t"
    "buffer_load_dwordx4 v106, s[44:47], s76 offen nt lds\n\t"
    "ds_read_b64_tr_b16 a[68:69], v99 offset:49152\n\t"
    "v_add_f32 v80, v80, v38\n\t"
    "v_exp_f32 v39, v39\n\t"
    "v_mfma_f32_32x32x16_bf16 v[16:31], a[12:15], v[140:143], v[16:31]\n\t"
    "ds_read_b128 a[4:7], v83 offset:32768\n\t"
    "ds_read_b64_tr_b16 a[70:71], v99 offset:51200\n\t"
    "v_cvt_pk_bf16_f32 v59, v38, v39\n\t"
    "v_add_f32 v80, v80, v39\n\t"
    "v_exp_f32 v40, v40\n\t"
    "v_mfma_f32_32x32x16_bf16 v[16:31], a[16:19], v[144:147], v[16:31]\n\t"
    "ds_read_b64_tr_b16 a[72:73], v100 offset:49152\n\t"
    "v_add_f32 v80, v80, v40\n\t"
    "v_exp_f32 v41, v41\n\t"
    "v_mfma_f32_32x32x16_bf16 v[16:31], a[20:23], v[148:151], v[16:31]\n\t"
    "ds_read_b128 a[8:11], v84 offset:32768\n\t"
    "ds_read_b64_tr_b16 a[74:75], v100 offset:51200\n\t"
    "v_cvt_pk_bf16_f32 v60, v40, v41\n\t"
    "v_add_f32 v80, v80, v41\n\t"
    "v_exp_f32 v42, v42\n\t"
    "v_mfma_f32_32x32x16_bf16 v[16:31], a[24:27], v[152:155], v[16:31]\n\t"
    "buffer_load_dwordx4 v107, s[44:47], s76 offen offset:1024 nt lds\n\t"
    "ds_read_b64_tr_b16 a[76:77], v101 offset:49152\n\t"
    "v_add_f32 v80, v80, v42\n\t"
    "v_exp_f32 v43, v43\n\t"
    "v_mfma_f32_32x32x16_bf16 v[16:31], a[28:31], v[156:159], v[16:31]\n\t"
    "ds_read_b128 a[12:15], v85 offset:32768\n\t"
    "ds_read_b64_tr_b16 a[78:79], v101 offset:51200\n\t"
    "v_cvt_pk_bf16_f32 v61, v42, v43\n\t"
    "v_add_f32 v80, v80, v43\n\t"
    "v_exp_f32 v44, v44\n\t"
    "s_waitcnt lgkmcnt(13)\n\t"
    "v_mfma_f32_32x32x16_bf16 v[160:175], a[96:99], v[56:59], v[160:175]\n\t"
    "ds_read_b64_tr_b16 a[80:81], v98 offset:53248\n\t"
    "v_add_f32 v80, v80, v44\n\t"
    "v_exp_f32 v45, v45\n\t"
    "v_mfma_f32_32x32x16_bf16 v[176:191], a[100:103], v[56:59], v[176:191]\n\t"
    "ds_read_b128 a[16:19], v86 offset:32768\n\t"
    "ds_read_b64_tr_b16 a[82:83], v98 offset:55296\n\t"
    "v_cvt_pk_bf16_f32 v62, v44, v45\n\t"
    "v_add_f32 v80, v80, v45\n\t"
    "v_exp_f32 v46, v46\n\t"
    "v_mfma_f32_32x32x16_bf16 v[192:207], a[104:107], v[56:59], v[192:207]\n\t"
    "buffer_load_dwordx4 v108, s[44:47], s76 offen offset:2048 nt lds\n\t"
    "ds_read_b64_tr_b16 a[84:85], v99 offset:53248\n\t"
    "v_add_f32 v80, v80, v46\n\t"
    "v_exp_f32 v47, v47\n\t"
    "v_mfma_f32_32x32x16_bf16 v[208:223], a[108:111], v[56:59], v[208:223]\n\t"
    "ds_read_b128 a[20:23], v87 offset:32768\n\t"
    "ds_read_b64_tr_b16 a[86:87], v99 offset:55296\n\t"
    "v_cvt_pk_bf16_f32 v63, v46, v47\n\t"
    "v_add_f32 v80, v80, v47\n\t"
    "v_exp_f32 v16, v16\n\t"
    "v_mfma_f32_32x32x16_bf16 v[160:175], a[112:115], v[60:63], v[160:175]\n\t"
    "ds_read_b64_tr_b16 a[88:89], v100 offset:53248\n\t"
    "v_add_f32 v80, v80, v16\n\t"
    "v_exp_f32 v17, v17\n\t"
    "v_mfma_f32_32x32x16_bf16 v[176:191], a[116:119], v[60:63], v[176:191]\n\t"
    "ds_read_b128 a[24:27], v88 offset:32768\n\t"
    "ds_read_b64_tr_b16 a[90:91], v100 offset:55296\n\t"
    "v_cvt_pk_bf16_f32 v48, v16, v17\n\t"
    "v_add_f32 v80, v80, v17\n\t"
    "v_exp_f32 v18, v18\n\t"
    "v_mfma_f32_32x32x16_bf16 v[192:207], a[120:123], v[60:63], v[192:207]\n\t"
    "buffer_load_dwordx4 v109, s[44:47], s76 offen offset:3072 nt lds\n\t"
    "ds_read_b64_tr_b16 a[92:93], v101 offset:53248\n\t"
    "v_add_f32 v80, v80, v18\n\t"
    "v_exp_f32 v19, v19\n\t"
    "v_mfma_f32_32x32x16_bf16 v[208:223], a[124:127], v[60:63], v[208:223]\n\t"
    "ds_read_b128 a[28:31], v89 offset:32768\n\t"
    "ds_read_b64_tr_b16 a[94:95], v101 offset:55296\n\t"
    "v_cvt_pk_bf16_f32 v49, v18, v19\n\t"
    "v_add_f32 v80, v80, v19\n\t"
    "v_exp_f32 v20, v20\n\t"
    "s_waitcnt lgkmcnt(15)\n\t"
    "v_mfma_f32_32x32x16_bf16 v[32:47], a[32:35], v[128:131], 0\n\t"
    "s_add_u32 s76, s75, 180224\n\t"
    "s_add_u32 m0, s75, 114688\n\t"
    "ds_read_b64_tr_b16 a[96:97], v98 offset:57344\n\t"
    "v_add_f32 v80, v80, v20\n\t"
    "v_exp_f32 v21, v21\n\t"
    "v_mfma_f32_32x32x16_bf16 v[32:47], a[36:39], v[132:135], v[32:47]\n\t"
    "ds_read_b64_tr_b16 a[98:99], v98 offset:59392\n\t"
    "ds_read_b128 a[32:35], v82 offset:40960\n\t"
    "v_cvt_pk_bf16_f32 v50, v20, v21\n\t"
    "v_add_f32 v80, v80, v21\n\t"
    "v_exp_f32 v22, v22\n\t"
    "v_mfma_f32_32x32x16_bf16 v[32:47], a[40:43], v[136:139], v[32:47]\n\t"
    "buffer_load_dwordx4 v110, s[48:51], s76 offen nt lds\n\t"
    "ds_read_b64_tr_b16 a[100:101], v99 offset:57344\n\t"
    "v_add_f32 v80, v80, v22\n\t"
    "v_exp_f32 v23, v23\n\t"
    "v_mfma_f32_32x32x16_bf16 v[32:47], a[44:47], v[140:143], v[32:47]\n\t"
    "ds_read_b64_tr_b16 a[102:103], v99 offset:59392\n\t"
    "ds_read_b128 a[36:39], v83 offset:40960\n\t"
    "v_cvt_pk_bf16_f32 v51, v22, v23\n\t"
    "v_add_f32 v80, v80, v23\n\t"
    "v_exp_f32 v24, v24\n\t"
    "v_mfma_f32_32x32x16_bf16 v[32:47], a[48:51], v[144:147], v[32:47]\n\t"
    "ds_read_b64_tr_b16 a[104:105], v100 offset:57344\n\t"
    "v_add_f32 v80, v80, v24\n\t"
    "v_exp_f32 v25, v25\n\t"
    "v_mfma_f32_32x32x16_bf16 v[32:47], a[52:55], v[148:151], v[32:47]\n\t"
    "ds_read_b64_tr_b16 a[106:107], v100 offset:59392\n\t"
    "ds_read_b128 a[40:43], v84 offset:40960\n\t"
    "v_cvt_pk_bf16_f32 v52, v24, v25\n\t"
    "v_add_f32 v80, v80, v25\n\t"
    "v_exp_f32 v26, v26\n\t"
    "v_mfma_f32_32x32x16_bf16 v[32:47], a[56:59], v[152:155], v[32:47]\n\t"
    "buffer_load_dwordx4 v110, s[48:51], s76 offen offset:1024 nt lds\n\t"
    "ds_read_b64_tr_b16 a[108:109], v101 offset:57344\n\t"
    "v_add_f32 v80, v80, v26\n\t"
    "v_exp_f32 v27, v27\n\t"
    "v_mfma_f32_32x32x16_bf16 v[32:47], a[60:63], v[156:159], v[32:47]\n\t"
    "ds_read_b64_tr_b16 a[110:111], v101 offset:59392\n\t"
    "ds_read_b128 a[44:47], v85 offset:40960\n\t"
    "v_cvt_pk_bf16_f32 v53, v26, v27\n\t"
    "v_add_f32 v80, v80, v27\n\t"
    "v_exp_f32 v28, v28\n\t"
    "s_waitcnt lgkmcnt(12)\n\t"
    "v_mfma_f32_32x32x16_bf16 v[160:175], a[64:67], v[48:51], v[160:175]\n\t"
    "ds_read_b64_tr_b16 a[112:113], v98 offset:61440\n\t"
    "v_add_f32 v80, v80, v28\n\t"
    "v_exp_f32 v29, v29\n\t"
    "v_mfma_f32_32x32x16_bf16 v[176:191], a[68:71], v[48:51], v[176:191]\n\t"
    "ds_read_b64_tr_b16 a[114:115], v98 offset:63488\n\t"
    "ds_read_b128 a[48:51], v86 offset:40960\n\t"
    "v_cvt_pk_bf16_f32 v54, v28, v29\n\t"
    "v_add_f32 v80, v80, v29\n\t"
    "v_exp_f32 v30, v30\n\t"
    "v_mfma_f32_32x32x16_bf16 v[192:207], a[72:75], v[48:51], v[192:207]\n\t"
    "buffer_load_dwordx4 v110, s[48:51], s76 offen offset:2048 nt lds\n\t"
    "ds_read_b64_tr_b16 a[116:117], v99 offset:61440\n\t"
    "v_add_f32 v80, v80, v30\n\t"
    "v_exp_f32 v31, v31\n\t"
    "v_mfma_f32_32x32x16_bf16 v[208:223], a[76:79], v[48:51], v[208:223]\n\t"
    "ds_read_b64_tr_b16 a[118:119], v99 offset:63488\n\t"
    "ds_read_b128 a[52:55], v87 offset:40960\n\t"
    "v_cvt_pk_bf16_f32 v55, v30, v31\n\t"
    "v_add_f32 v80, v80, v31\n\t"
    "v_exp_f32 v32, v32\n\t"
    "v_mfma_f32_32x32x16_bf16 v[160:175], a[80:83], v[52:55], v[160:175]\n\t"
    "ds_read_b64_tr_b16 a[120:121], v100 offset:61440\n\t"
    "v_add_f32 v80, v80, v32\n\t"
    "v_exp_f32 v33, v33\n\t"
    "v_mfma_f32_32x32x16_bf16 v[176:191], a[84:87], v[52:55], v[176:191]\n\t"
    "ds_read_b64_tr_b16 a[122:123], v100 offset:63488\n\t"
    "ds_read_b128 a[56:59], v88 offset:40960\n\t"
    "v_cvt_pk_bf16_f32 v56, v32, v33\n\t"
    "v_add_f32 v80, v80, v33\n\t"
    "v_exp_f32 v34, v34\n\t"
    "v_mfma_f32_32x32x16_bf16 v[192:207], a[88:91], v[52:55], v[192:207]\n\t"
    "buffer_load_dwordx4 v110, s[48:51], s76 offen offset:3072 nt lds\n\t"
    "ds_read_b64_tr_b16 a[124:125], v101 offset:61440\n\t"
    "v_add_f32 v80, v80, v34\n\t"
    "v_exp_f32 v35, v35\n\t"
    "v_mfma_f32_32x32x16_bf16 v[208:223], a[92:95], v[52:55], v[208:223]\n\t"
    "ds_read_b64_tr_b16 a[126:127], v101 offset:63488\n\t"
    "ds_read_b128 a[60:63], v89 offset:40960\n\t"
    "v_cvt_pk_bf16_f32 v57, v34, v35\n\t"
    "v_add_f32 v80, v80, v35\n\t"
    "v_exp_f32 v36, v36\n\t"
    "s_waitcnt vmcnt(8)\n\t"
    "s_barrier\n\t"
    "v_mfma_f32_32x32x16_bf16 v[16:31], a[0:3], v[128:131], 0\n\t"
    "s_add_u32 s76, s75, 212992\n\t"
    "s_add_u32 m0, s75, 0\n\t"
    "ds_read_b64_tr_b16 a[64:65], v102 offset:16384\n\t"
    "v_add_f32 v80, v80, v36\n\t"
    "v_exp_f32 v37, v37\n\t"
    "v_mfma_f32_32x32x16_bf16 v[16:31], a[4:7], v[132:135], v[16:31]\n\t"
    "ds_read_b128 a[0:3], v90\n\t"
    "ds_read_b64_tr_b16 a[66:67], v102 offset:18432\n\t"
    "v_cvt_pk_bf16_f32 v58, v36, v37\n\t"
    "v_add_f32 v80, v80, v37\n\t"
    "v_exp_f32 v38, v38\n\t"
    "v_mfma_f32_32x32x16_bf16 v[16:31], a[8:11], v[136:139], v[16:31]\n\t"
    "buffer_load_dwordx4 v106, s[44:47], s76 offen nt lds\n\t"
    "ds_read_b64_tr_b16 a[68:69], v103 offset:16384\n\t"
    "v_add_f32 v80, v80, v38\n\t"
    "v_exp_f32 v39, v39\n\t"
    "v_mfma_f32_32x32x16_bf16 v[16:31], a[12:15], v[140:143], v[16:31]\n\t"
    "ds_read_b128 a[4:7], v91\n\t"
    "ds_read_b64_tr_b16 a[70:71], v103 offset:18432\n\t"
    "v_cvt_pk_bf16_f32 v59, v38, v39\n\t"
    "v_add_f32 v80, v80, v39\n\t"
    "v_exp_f32 v40, v40\n\t"
    "v_mfma_f32_32x32x16_bf16 v[16:31], a[16:19], v[144:147], v[16:31]\n\t"
    "ds_read_b64_tr_b16 a[72:73], v104 offset:16384\n\t"
    "v_add_f32 v80, v80, v40\n\t"
    "v_exp_f32 v41, v41\n\t"
    "v_mfma_f32_32x32x16_bf16 v[16:31], a[20:23], v[148:151], v[16:31]\n\t"
    "ds_read_b128 a[8:11], v92\n\t"
    "ds_read_b64_tr_b16 a[74:75], v104 offset:18432\n\t"
    "v_cvt_pk_bf16_f32 v60, v40, v41\n\t"
    "v_add_f32 v80, v80, v41\n\t"
    "v_exp_f32 v42, v42\n\t"
    "v_mfma_f32_32x32x16_bf16 v[16:31], a[24:27], v[152:155], v[16:31]\n\t"
    "buffer_load_dwordx4 v107, s[44:47], s76 offen offset:1024 nt lds\n\t"
    "ds_read_b64_tr_b16 a[76:77], v105 offset:16384\n\t"
    "v_add_f32 v80, v80, v42\n\t"
    "v_exp_f32 v43, v43\n\t"
    "v_mfma_f32_32x32x16_bf16 v[16:31], a[28:31], v[156:159], v[16:31]\n\t"
    "ds_read_b128 a[12:15], v93\n\t"
    "ds_read_b64_tr_b16 a[78:79], v105 offset:18432\n\t"
    "v_cvt_pk_bf16_f32 v61, v42, v43\n\t"
    "v_add_f32 v80, v80, v43\n\t"
    "v_exp_f32 v44, v44\n\t"
    "s_waitcnt lgkmcnt(13)\n\t"
    "v_mfma_f32_32x32x16_bf16 v[160:175], a[96:99], v[56:59], v[160:175]\n\t"
    "ds_read_b64_tr_b16 a[80:81], v102 offset:20480\n\t"
    "v_add_f32 v80, v80, v44\n\t"
    "v_exp_f32 v45, v45\n\t"
    "v_mfma_f32_32x32x16_bf16 v[176:191], a[100:103], v[56:59], v[176:191]\n\t"
    "ds_read_b128 a[16:19], v94\n\t"
    "ds_read_b64_tr_b16 a[82:83], v102 offset:22528\n\t"
    "v_cvt_pk_bf16_f32 v62, v44, v45\n\t"
    "v_add_f32 v80, v80, v45\n\t"
    "v_exp_f32 v46, v46\n\t"
    "v_mfma_f32_32x32x16_bf16 v[192:207], a[104:107], v[56:59], v[192:207]\n\t"
    "buffer_load_dwordx4 v108, s[44:47], s76 offen offset:2048 nt lds\n\t"
    "ds_read_b64_tr_b16 a[84:85], v103 offset:20480\n\t"
    "v_add_f32 v80, v80, v46\n\t"
    "v_exp_f32 v47, v47\n\t"
    "v_mfma_f32_32x32x16_bf16 v[208:223], a[108:111], v[56:59], v[208:223]\n\t"
    "ds_read_b128 a[20:23], v95\n\t"
    "ds_read_b64_tr_b16 a[86:87], v103 offset:22528\n\t"
    "v_cvt_pk_bf16_f32 v63, v46, v47\n\t"
    "v_add_f32 v80, v80, v47\n\t"
    "v_exp_f32 v16, v16\n\t"
    "v_mfma_f32_32x32x16_bf16 v[160:175], a[112:115], v[60:63], v[160:175]\n\t"
    "ds_read_b64_tr_b16 a[88:89], v104 offset:20480\n\t"
    "v_add_f32 v80, v80, v16\n\t"
    "v_exp_f32 v17, v17\n\t"
    "v_mfma_f32_32x32x16_bf16 v[176:191], a[116:119], v[60:63], v[176:191]\n\t"
    "ds_read_b128 a[24:27], v96\n\t"
    "ds_read_b64_tr_b16 a[90:91], v104 offset:22528\n\t"
    "v_cvt_pk_bf16_f32 v48, v16, v17\n\t"
    "v_add_f32 v80, v80, v17\n\t"
    "v_exp_f32 v18, v18\n\t"
    "v_mfma_f32_32x32x16_bf16 v[192:207], a[120:123], v[60:63], v[192:207]\n\t"
    "buffer_load_dwordx4 v109, s[44:47], s76 offen offset:3072 nt lds\n\t"
    "ds_read_b64_tr_b16 a[92:93], v105 offset:20480\n\t"
    "v_add_f32 v80, v80, v18\n\t"
    "v_exp_f32 v19, v19\n\t"
    "v_mfma_f32_32x32x16_bf16 v[208:223], a[124:127], v[60:63], v[208:223]\n\t"
    "ds_read_b128 a[28:31], v97\n\t"
    "ds_read_b64_tr_b16 a[94:95], v105 offset:22528\n\t"
    "v_cvt_pk_bf16_f32 v49, v18, v19\n\t"
    "v_add_f32 v80, v80, v19\n\t"
    "v_exp_f32 v20, v20\n\t"
    "s_waitcnt lgkmcnt(15)\n\t"
    "v_mfma_f32_32x32x16_bf16 v[32:47], a[32:35], v[128:131], 0\n\t"
    "s_add_u32 s76, s75, 196608\n\t"
    "s_add_u32 m0, s75, 16384\n\t"
    "ds_read_b64_tr_b16 a[96:97], v102 offset:24576\n\t"
    "v_add_f32 v80, v80, v20\n\t"
    "v_exp_f32 v21, v21\n\t"
    "v_mfma_f32_32x32x16_bf16 v[32:47], a[36:39], v[132:135], v[32:47]\n\t"
    "ds_read_b64_tr_b16 a[98:99], v102 offset:26624\n\t"
    "ds_read_b128 a[32:35], v90 offset:8192\n\t"
    "v_cvt_pk_bf16_f32 v50, v20, v21\n\t"
    "v_add_f32 v80, v80, v21\n\t"
    "v_exp_f32 v22, v22\n\t"
    "v_mfma_f32_32x32x16_bf16 v[32:47], a[40:43], v[136:139], v[32:47]\n\t"
    "buffer_load_dwordx4 v110, s[48:51], s76 offen nt lds\n\t"
    "ds_read_b64_tr_b16 a[100:101], v103 offset:24576\n\t"
    "v_add_f32 v80, v80, v22\n\t"
    "v_exp_f32 v23, v23\n\t"
    "v_mfma_f32_32x32x16_bf16 v[32:47], a[44:47], v[140:143], v[32:47]\n\t"
    "ds_read_b64_tr_b16 a[102:103], v103 offset:26624\n\t"
    "ds_read_b128 a[36:39], v91 offset:8192\n\t"
    "v_cvt_pk_bf16_f32 v51, v22, v23\n\t"
    "v_add_f32 v80, v80, v23\n\t"
    "v_exp_f32 v24, v24\n\t"
    "v_mfma_f32_32x32x16_bf16 v[32:47], a[48:51], v[144:147], v[32:47]\n\t"
    "ds_read_b64_tr_b16 a[104:105], v104 offset:24576\n\t"
    "v_add_f32 v80, v80, v24\n\t"
    "v_exp_f32 v25, v25\n\t"
    "v_mfma_f32_32x32x16_bf16 v[32:47], a[52:55], v[148:151], v[32:47]\n\t"
    "ds_read_b64_tr_b16 a[106:107], v104 offset:26624\n\t"
    "ds_read_b128 a[40:43], v92 offset:8192\n\t"
    "v_cvt_pk_bf16_f32 v52, v24, v25\n\t"
    "v_add_f32 v80, v80, v25\n\t"
    "v_exp_f32 v26, v26\n\t"
    "v_mfma_f32_32x32x16_bf16 v[32:47], a[56:59], v[152:155], v[32:47]\n\t"
    "buffer_load_dwordx4 v110, s[48:51], s76 offen offset:1024 nt lds\n\t"
    "ds_read_b64_tr_b16 a[108:109], v105 offset:24576\n\t"
    "v_add_f32 v80, v80, v26\n\t"
    "v_exp_f32 v27, v27\n\t"
    "v_mfma_f32_32x32x16_bf16 v[32:47], a[60:63], v[156:159], v[32:47]\n\t"
    "ds_read_b64_tr_b16 a[110:111], v105 offset:26624\n\t"
    "ds_read_b128 a[44:47], v93 offset:8192\n\t"
    "v_cvt_pk_bf16_f32 v53, v26, v27\n\t"
    "v_add_f32 v80, v80, v27\n\t"
    "v_exp_f32 v28, v28\n\t"
    "s_waitcnt lgkmcnt(12)\n\t"
    "v_mfma_f32_32x32x16_bf16 v[160:175], a[64:67], v[48:51], v[160:175]\n\t"
    "ds_read_b64_tr_b16 a[112:113], v102 offset:28672\n\t"
    "v_add_f32 v80, v80, v28\n\t"
    "v_exp_f32 v29, v29\n\t"
    "v_mfma_f32_32x32x16_bf16 v[176:191], a[68:71], v[48:51], v[176:191]\n\t"
    "ds_read_b64_tr_b16 a[114:115], v102 offset:30720\n\t"
    "ds_read_b128 a[48:51], v94 offset:8192\n\t"
    "v_cvt_pk_bf16_f32 v54, v28, v29\n\t"
    "v_add_f32 v80, v80, v29\n\t"
    "v_exp_f32 v30, v30\n\t"
    "v_mfma_f32_32x32x16_bf16 v[192:207], a[72:75], v[48:51], v[192:207]\n\t"
    "buffer_load_dwordx4 v110, s[48:51], s76 offen offset:2048 nt lds\n\t"
    "ds_read_b64_tr_b16 a[116:117], v103 offset:28672\n\t"
    "v_add_f32 v80, v80, v30\n\t"
    "v_exp_f32 v31, v31\n\t"
    "v_mfma_f32_32x32x16_bf16 v[208:223], a[76:79], v[48:51], v[208:223]\n\t"
    "ds_read_b64_tr_b16 a[118:119], v103 offset:30720\n\t"
    "ds_read_b128 a[52:55], v95 offset:8192\n\t"
    "v_cvt_pk_bf16_f32 v55, v30, v31\n\t"
    "v_add_f32 v80, v80, v31\n\t"
    "v_exp_f32 v32, v32\n\t"
    "v_mfma_f32_32x32x16_bf16 v[160:175], a[80:83], v[52:55], v[160:175]\n\t"
    "ds_read_b64_tr_b16 a[120:121], v104 offset:28672\n\t"
    "v_add_f32 v80, v80, v32\n\t"
    "v_exp_f32 v33, v33\n\t"
    "v_mfma_f32_32x32x16_bf16 v[176:191], a[84:87], v[52:55], v[176:191]\n\t"
    "ds_read_b64_tr_b16 a[122:123], v104 offset:30720\n\t"
    "ds_read_b128 a[56:59], v96 offset:8192\n\t"
    "v_cvt_pk_bf16_f32 v56, v32, v33\n\t"
    "v_add_f32 v80, v80, v33\n\t"
    "v_exp_f32 v34, v34\n\t"
    "v_mfma_f32_32x32x16_bf16 v[192:207], a[88:91], v[52:55], v[192:207]\n\t"
    "buffer_load_dwordx4 v110, s[48:51], s76 offen offset:3072 nt lds\n\t"
    "ds_read_b64_tr_b16 a[124:125], v105 offset:28672\n\t"
    "v_add_f32 v80, v80, v34\n\t"
    "v_exp_f32 v35, v35\n\t"
    "v_mfma_f32_32x32x16_bf16 v[208:223], a[92:95], v[52:55], v[208:223]\n\t"
    "ds_read_b64_tr_b16 a[126:127], v105 offset:30720\n\t"
    "ds_read_b128 a[60:63], v97 offset:8192\n\t"
    "v_cvt_pk_bf16_f32 v57, v34, v35\n\t"
    "v_add_f32 v80, v80, v35\n\t"
    "v_exp_f32 v36, v36\n\t"
    "s_waitcnt vmcnt(8)\n\t"
    "s_barrier\n\t"
    "v_mfma_f32_32x32x16_bf16 v[16:31], a[0:3], v[128:131], 0\n\t"
    "s_add_u32 s76, s75, 229376\n\t"
    "s_add_u32 m0, s75, 32768\n\t"
    "ds_read_b64_tr_b16 a[64:65], v102 offset:49152\n\t"
    "v_add_f32 v80, v80, v36\n\t"
    "v_exp_f32 v37, v37\n\t"
    "v_mfma_f32_32x32x16_bf16 v[16:31], a[4:7], v[132:135], v[16:31]\n\t"
    "ds_read_b128 a[0:3], v90 offset:32768\n\t"
    "ds_read_b64_tr_b16 a[66:67], v102 offset:51200\n\t"
    "v_cvt_pk_bf16_f32 v58, v36, v37\n\t"
    "v_add_f32 v80, v80, v37\n\t"
    "v_exp_f32 v38, v38\n\t"
    "v_mfma_f32_32x32x16_bf16 v[16:31], a[8:11], v[136:139], v[16:31]\n\t"
    "buffer_load_dwordx4 v106, s[44:47], s76 offen nt lds\n\t"
    "ds_read_b64_tr_b16 a[68:69], v103 offset:49152\n\t"
    "v_add_f32 v80, v80, v38\n\t"
    "v_exp_f32 v39, v39\n\t"
    "v_mfma_f32_32x32x16_bf16 v[16:31], a[12:15], v[140:143], v[16:31]\n\t"
    "ds_read_b128 a[4:7], v91 offset:32768\n\t"
    "ds_read_b64_tr_b16 a[70:71], v103 offset:51200\n\t"
    "v_cvt_pk_bf16_f32 v59, v38, v39\n\t"
    "v_add_f32 v80, v80, v39\n\t"
    "v_exp_f32 v40, v40\n\t"
    "v_mfma_f32_32x32x16_bf16 v[16:31], a[16:19], v[144:147], v[16:31]\n\t"
    "ds_read_b64_tr_b16 a[72:73], v104 offset:49152\n\t"
    "v_add_f32 v80, v80, v40\n\t"
    "v_exp_f32 v41, v41\n\t"
    "v_mfma_f32_32x32x16_bf16 v[16:31], a[20:23], v[148:151], v[16:31]\n\t"
    "ds_read_b128 a[8:11], v92 offset:32768\n\t"
    "ds_read_b64_tr_b16 a[74:75], v104 offset:51200\n\t"
    "v_cvt_pk_bf16_f32 v60, v40, v41\n\t"
    "v_add_f32 v80, v80, v41\n\t"
    "v_exp_f32 v42, v42\n\t"
    "v_mfma_f32_32x32x16_bf16 v[16:31], a[24:27], v[152:155], v[16:31]\n\t"
    "buffer_load_dwordx4 v107, s[44:47], s76 offen offset:1024 nt lds\n\t"
    "ds_read_b64_tr_b16 a[76:77], v105 offset:49152\n\t"
    "v_add_f32 v80, v80, v42\n\t"
    "v_exp_f32 v43, v43\n\t"
    "v_mfma_f32_32x32x16_bf16 v[16:31], a[28:31], v[156:159], v[16:31]\n\t"
    "ds_read_b128 a[12:15], v93 offset:32768\n\t"
    "ds_read_b64_tr_b16 a[78:79], v105 offset:51200\n\t"
    "v_cvt_pk_bf16_f32 v61, v42, v43\n\t"
    "v_add_f32 v80, v80, v43\n\t"
    "v_exp_f32 v44, v44\n\t"
    "s_waitcnt lgkmcnt(13)\n\t"
    "v_mfma_f32_32x32x16_bf16 v[160:175], a[96:99], v[56:59], v[160:175]\n\t"
    "ds_read_b64_tr_b16 a[80:81], v102 offset:53248\n\t"
    "v_add_f32 v80, v80, v44\n\t"
    "v_exp_f32 v45, v45\n\t"
    "v_mfma_f32_32x32x16_bf16 v[176:191], a[100:103], v[56:59], v[176:191]\n\t"
    "ds_read_b128 a[16:19], v94 offset:32768\n\t"
    "ds_read_b64_tr_b16 a[82:83], v102 offset:55296\n\t"
    "v_cvt_pk_bf16_f32 v62, v44, v45\n\t"
    "v_add_f32 v80, v80, v45\n\t"
    "v_exp_f32 v46, v46\n\t"
    "v_mfma_f32_32x32x16_bf16 v[192:207], a[104:107], v[56:59], v[192:207]\n\t"
    "buffer_load_dwordx4 v108, s[44:47], s76 offen offset:2048 nt lds\n\t"
    "ds_read_b64_tr_b16 a[84:85], v103 offset:53248\n\t"
    "v_add_f32 v80, v80, v46\n\t"
    "v_exp_f32 v47, v47\n\t"
    "v_mfma_f32_32x32x16_bf16 v[208:223], a[108:111], v[56:59], v[208:223]\n\t"
    "ds_read_b128 a[20:23], v95 offset:32768\n\t"
    "ds_read_b64_tr_b16 a[86:87], v103 offset:55296\n\t"
    "v_cvt_pk_bf16_f32 v63, v46, v47\n\t"
    "v_add_f32 v80, v80, v47\n\t"
    "v_exp_f32 v16, v16\n\t"
    "v_mfma_f32_32x32x16_bf16 v[160:175], a[112:115], v[60:63], v[160:175]\n\t"
    "ds_read_b64_tr_b16 a[88:89], v104 offset:53248\n\t"
    "v_add_f32 v80, v80, v16\n\t"
    "v_exp_f32 v17, v17\n\t"
    "v_mfma_f32_32x32x16_bf16 v[176:191], a[116:119], v[60:63], v[176:191]\n\t"
    "ds_read_b128 a[24:27], v96 offset:32768\n\t"
    "ds_read_b64_tr_b16 a[90:91], v104 offset:55296\n\t"
    "v_cvt_pk_bf16_f32 v48, v16, v17\n\t"
    "v_add_f32 v80, v80, v17\n\t"
    "v_exp_f32 v18, v18\n\t"
    "v_mfma_f32_32x32x16_bf16 v[192:207], a[120:123], v[60:63], v[192:207]\n\t"
    "buffer_load_dwordx4 v109, s[44:47], s76 offen offset:3072 nt lds\n\t"
    "ds_read_b64_tr_b16 a[92:93], v105 offset:53248\n\t"
    "v_add_f32 v80, v80, v18\n\t"
    "v_exp_f32 v19, v19\n\t"
    "v_mfma_f32_32x32x16_bf16 v[208:223], a[124:127], v[60:63], v[208:223]\n\t"
    "ds_read_b128 a[28:31], v97 offset:32768\n\t"
    "ds_read_b64_tr_b16 a[94:95], v105 offset:55296\n\t"
    "v_cvt_pk_bf16_f32 v49, v18, v19\n\t"
    "v_add_f32 v80, v80, v19\n\t"
    "v_exp_f32 v20, v20\n\t"
    "s_waitcnt lgkmcnt(15)\n\t"
    "v_mfma_f32_32x32x16_bf16 v[32:47], a[32:35], v[128:131], 0\n\t"
    "s_add_u32 s76, s75, 212992\n\t"
    "s_add_u32 m0, s75, 49152\n\t"
    "ds_read_b64_tr_b16 a[96:97], v102 offset:57344\n\t"
    "v_add_f32 v80, v80, v20\n\t"
    "v_exp_f32 v21, v21\n\t"
    "v_mfma_f32_32x32x16_bf16 v[32:47], a[36:39], v[132:135], v[32:47]\n\t"
    "ds_read_b64_tr_b16 a[98:99], v102 offset:59392\n\t"
    "ds_read_b128 a[32:35], v90 offset:40960\n\t"
    "v_cvt_pk_bf16_f32 v50, v20, v21\n\t"
    "v_add_f32 v80, v80, v21\n\t"
    "v_exp_f32 v22, v22\n\t"
    "v_mfma_f32_32x32x16_bf16 v[32:47], a[40:43], v[136:139], v[32:47]\n\t"
    "buffer_load_dwordx4 v110, s[48:51], s76 offen nt lds\n\t"
    "ds_read_b64_tr_b16 a[100:101], v103 offset:57344\n\t"
    "v_add_f32 v80, v80, v22\n\t"
    "v_exp_f32 v23, v23\n\t"
    "v_mfma_f32_32x32x16_bf16 v[32:47], a[44:47], v[140:143], v[32:47]\n\t"
    "ds_read_b64_tr_b16 a[102:103], v103 offset:59392\n\t"
    "ds_read_b128 a[36:39], v91 offset:40960\n\t"
    "v_cvt_pk_bf16_f32 v51, v22, v23\n\t"
    "v_add_f32 v80, v80, v23\n\t"
    "v_exp_f32 v24, v24\n\t"
    "v_mfma_f32_32x32x16_bf16 v[32:47], a[48:51], v[144:147], v[32:47]\n\t"
    "ds_read_b64_tr_b16 a[104:105], v104 offset:57344\n\t"
    "v_add_f32 v80, v80, v24\n\t"
    "v_exp_f32 v25, v25\n\t"
    "v_mfma_f32_32x32x16_bf16 v[32:47], a[52:55], v[148:151], v[32:47]\n\t"
    "ds_read_b64_tr_b16 a[106:107], v104 offset:59392\n\t"
    "ds_read_b128 a[40:43], v92 offset:40960\n\t"
    "v_cvt_pk_bf16_f32 v52, v24, v25\n\t"
    "v_add_f32 v80, v80, v25\n\t"
    "v_exp_f32 v26, v26\n\t"
    "v_mfma_f32_32x32x16_bf16 v[32:47], a[56:59], v[152:155], v[32:47]\n\t"
    "buffer_load_dwordx4 v110, s[48:51], s76 offen offset:1024 nt lds\n\t"
    "ds_read_b64_tr_b16 a[108:109], v105 offset:57344\n\t"
    "v_add_f32 v80, v80, v26\n\t"
    "v_exp_f32 v27, v27\n\t"
    "v_mfma_f32_32x32x16_bf16 v[32:47], a[60:63], v[156:159], v[32:47]\n\t"
    "ds_read_b64_tr_b16 a[110:111], v105 offset:59392\n\t"
    "ds_read_b128 a[44:47], v93 offset:40960\n\t"
    "v_cvt_pk_bf16_f32 v53, v26, v27\n\t"
    "v_add_f32 v80, v80, v27\n\t"
    "v_exp_f32 v28, v28\n\t"
    "s_waitcnt lgkmcnt(12)\n\t"
    "v_mfma_f32_32x32x16_bf16 v[160:175], a[64:67], v[48:51], v[160:175]\n\t"
    "ds_read_b64_tr_b16 a[112:113], v102 offset:61440\n\t"
    "v_add_f32 v80, v80, v28\n\t"
    "v_exp_f32 v29, v29\n\t"
    "v_mfma_f32_32x32x16_bf16 v[176:191], a[68:71], v[48:51], v[176:191]\n\t"
    "ds_read_b64_tr_b16 a[114:115], v102 offset:63488\n\t"
    "ds_read_b128 a[48:51], v94 offset:40960\n\t"
    "v_cvt_pk_bf16_f32 v54, v28, v29\n\t"
    "v_add_f32 v80, v80, v29\n\t"
    "v_exp_f32 v30, v30\n\t"
    "v_mfma_f32_32x32x16_bf16 v[192:207], a[72:75], v[48:51], v[192:207]\n\t"
    "buffer_load_dwordx4 v110, s[48:51], s76 offen offset:2048 nt lds\n\t"
    "ds_read_b64_tr_b16 a[116:117], v103 offset:61440\n\t"
    "v_add_f32 v80, v80, v30\n\t"
    "v_exp_f32 v31, v31\n\t"
    "v_mfma_f32_32x32x16_bf16 v[208:223], a[76:79], v[48:51], v[208:223]\n\t"
    "ds_read_b64_tr_b16 a[118:119], v103 offset:63488\n\t"
    "ds_read_b128 a[52:55], v95 offset:40960\n\t"
    "v_cvt_pk_bf16_f32 v55, v30, v31\n\t"
    "v_add_f32 v80, v80, v31\n\t"
    "v_exp_f32 v32, v32\n\t"
    "v_mfma_f32_32x32x16_bf16 v[160:175], a[80:83], v[52:55], v[160:175]\n\t"
    "ds_read_b64_tr_b16 a[120:121], v104 offset:61440\n\t"
    "v_add_f32 v80, v80, v32\n\t"
    "v_exp_f32 v33, v33\n\t"
    "s_lshl_b32 s26, s22, 19\n\t"
    "s_mov_b64 s[72:73], %4\n\t"
    "s_add_u32 s72, s72, s26\n\t"
    "s_addc_u32 s73, s73, 0\n\t"
    "s_mov_b32 s36, s72\n\t"
    "s_and_b32 s37, s73, 0xffff\n\t"
    "s_mov_b32 s38, 0x80000\n\t"
    "s_mov_b32 s39, 0x20000\n\t"
    "s_lshl_b32 s26, s25, 14\n\t"
    "s_cmp_lt_u32 s21, 2\n\t"
    "s_cselect_b32 s26, s26, 0x7fff0000\n\t"
    "buffer_load_dword v81, v121, s[36:39], s26 offen\n\t"
    "v_mfma_f32_32x32x16_bf16 v[176:191], a[84:87], v[52:55], v[176:191]\n\t"
    "ds_read_b64_tr_b16 a[122:123], v104 offset:63488\n\t"
    "ds_read_b128 a[56:59], v96 offset:40960\n\t"
    "v_cvt_pk_bf16_f32 v56, v32, v33\n\t"
    "v_add_f32 v80, v80, v33\n\t"
    "v_exp_f32 v34, v34\n\t"
    "v_mfma_f32_32x32x16_bf16 v[192:207], a[88:91], v[52:55], v[192:207]\n\t"
    "buffer_load_dwordx4 v110, s[48:51], s76 offen offset:3072 nt lds\n\t"
    "ds_read_b64_tr_b16 a[124:125], v105 offset:61440\n\t"
    "v_add_f32 v80, v80, v34\n\t"
    "v_exp_f32 v35, v35\n\t"
    "v_mfma_f32_32x32x16_bf16 v[208:223], a[92:95], v[52:55], v[208:223]\n\t"
    "ds_read_b64_tr_b16 a[126:127], v105 offset:63488\n\t"
    "ds_read_b128 a[60:63], v97 offset:40960\n\t"
    "v_cvt_pk_bf16_f32 v57, v34, v35\n\t"
    "v_add_f32 v80, v80, v35\n\t"
    "v_exp_f32 v36, v36\n\t"
    "s_waitcnt vmcnt(9)\n\t"
    "s_barrier\n\t"
    "v_mfma_f32_32x32x16_bf16 v[16:31], a[0:3], v[128:131], 0\n\t"
    "s_add_u32 s76, s75, 245760\n\t"
    "s_add_u32 m0, s75, 65536\n\t"
    "ds_read_b64_tr_b16 a[64:65], v98 offset:16384\n\t"
    "v_add_f32 v80, v80, v36\n\t"
    "v_exp_f32 v37, v37\n\t"
    "v_mfma_f32_32x32x16_bf16 v[16:31], a[4:7], v[132:135], v[16:31]\n\t"
    "ds_read_b128 a[0:3], v82\n\t"
    "ds_read_b64_tr_b16 a[66:67], v98 offset:18432\n\t"
    "v_cvt_pk_bf16_f32 v58, v36, v37\n\t"
    "v_add_f32 v80, v80, v37\n\t"
    "v_exp_f32 v38, v38\n\t"
    "v_mfma_f32_32x32x16_bf16 v[16:31], a[8:11], v[136:139], v[16:31]\n\t"
    "buffer_load_dwordx4 v106, s[44:47], s76 offen nt lds\n\t"
    "ds_read_b64_tr_b16 a[68:69], v99 offset:16384\n\t"
    "v_add_f32 v80, v80, v38\n\t"
    "v_exp_f32 v39, v39\n\t"
    "v_mfma_f32_32x32x16_bf16 v[16:31], a[12:15], v[140:143], v[16:31]\n\t"
    "ds_read_b128 a[4:7], v83\n\t"
    "ds_read_b64_tr_b16 a[70:71], v99 offset:18432\n\t"
    "v_cvt_pk_bf16_f32 v59, v38, v39\n\t"
    "v_add_f32 v80, v80, v39\n\t"
    "v_exp_f32 v40, v40\n\t"
    "v_mfma_f32_32x32x16_bf16 v[16:31], a[16:19], v[144:147], v[16:31]\n\t"
    "ds_read_b64_tr_b16 a[72:73], v100 offset:16384\n\t"
    "v_add_f32 v80, v80, v40\n\t"
    "v_exp_f32 v41, v41\n\t"
    "v_mfma_f32_32x32x16_bf16 v[16:31], a[20:23], v[148:151], v[16:31]\n\t"
    "ds_read_b128 a[8:11], v84\n\t"
    "ds_read_b64_tr_b16 a[74:75], v100 offset:18432\n\t"
    "v_cvt_pk_bf16_f32 v60, v40, v41\n\t"
    "v_add_f32 v80, v80, v41\n\t"
    "v_exp_f32 v42, v42\n\t"
    "v_mfma_f32_32x32x16_bf16 v[16:31], a[24:27], v[152:155], v[16:31]\n\t"
    "buffer_load_dwordx4 v107, s[44:47], s76 offen offset:1024 nt lds\n\t"
    "ds_read_b64_tr_b16 a[76:77], v101 offset:16384\n\t"
    "v_add_f32 v80, v80, v42\n\t"
    "v_exp_f32 v43, v43\n\t"
    "v_mfma_f32_32x32x16_bf16 v[16:31], a[28:31], v[156:159], v[16:31]\n\t"
    "ds_read_b128 a[12:15], v85\n\t"
    "ds_read_b64_tr_b16 a[78:79], v101 offset:18432\n\t"
    "v_cvt_pk_bf16_f32 v61, v42, v43\n\t"
    "v_add_f32 v80, v80, v43\n\t"
    "v_exp_f32 v44, v44\n\t"
    "s_waitcnt lgkmcnt(13)\n\t"
    "v_mfma_f32_32x32x16_bf16 v[160:175], a[96:99], v[56:59], v[160:175]\n\t"
    "ds_read_b64_tr_b16 a[80:81], v98 offset:20480\n\t"
    "v_add_f32 v80, v80, v44\n\t"
    "v_exp_f32 v45, v45\n\t"
    "v_mfma_f32_32x32x16_bf16 v[176:191], a[100:103], v[56:59], v[176:191]\n\t"
    "ds_read_b128 a[16:19], v86\n\t"
    "ds_read_b64_tr_b16 a[82:83], v98 offset:22528\n\t"
    "v_cvt_pk_bf16_f32 v62, v44, v45\n\t"
    "v_add_f32 v80, v80, v45\n\t"
    "v_exp_f32 v46, v46\n\t"
    "v_mfma_f32_32x32x16_bf16 v[192:207], a[104:107], v[56:59], v[192:207]\n\t"
    "buffer_load_dwordx4 v108, s[44:47], s76 offen offset:2048 nt lds\n\t"
    "ds_read_b64_tr_b16 a[84:85], v99 offset:20480\n\t"
    "v_add_f32 v80, v80, v46\n\t"
    "v_exp_f32 v47, v47\n\t"
    "v_mfma_f32_32x32x16_bf16 v[208:223], a[108:111], v[56:59], v[208:223]\n\t"
    "ds_read_b128 a[20:23], v87\n\t"
    "ds_read_b64_tr_b16 a[86:87], v99 offset:22528\n\t"
    "v_cvt_pk_bf16_f32 v63, v46, v47\n\t"
    "v_add_f32 v80, v80, v47\n\t"
    "v_exp_f32 v16, v16\n\t"
    "v_mfma_f32_32x32x16_bf16 v[160:175], a[112:115], v[60:63], v[160:175]\n\t"
    "ds_read_b64_tr_b16 a[88:89], v100 offset:20480\n\t"
    "v_add_f32 v80, v80, v16\n\t"
    "v_exp_f32 v17, v17\n\t"
    "v_mfma_f32_32x32x16_bf16 v[176:191], a[116:119], v[60:63], v[176:191]\n\t"
    "ds_read_b128 a[24:27], v88\n\t"
    "ds_read_b64_tr_b16 a[90:91], v100 offset:22528\n\t"
    "v_cvt_pk_bf16_f32 v48, v16, v17\n\t"
    "v_add_f32 v80, v80, v17\n\t"
    "v_exp_f32 v18, v18\n\t"
    "v_mfma_f32_32x32x16_bf16 v[192:207], a[120:123], v[60:63], v[192:207]\n\t"
    "buffer_load_dwordx4 v109, s[44:47], s76 offen offset:3072 nt lds\n\t"
    "ds_read_b64_tr_b16 a[92:93], v101 offset:20480\n\t"
    "v_add_f32 v80, v80, v18\n\t"
    "v_exp_f32 v19, v19\n\t"
    "v_mfma_f32_32x32x16_bf16 v[208:223], a[124:127], v[60:63], v[208:223]\n\t"
    "ds_read_b128 a[28:31], v89\n\t"
    "ds_read_b64_tr_b16 a[94:95], v101 offset:22528\n\t"
    "v_cvt_pk_bf16_f32 v49, v18, v19\n\t"
    "v_add_f32 v80, v80, v19\n\t"
    "v_exp_f32 v20, v20\n\t"
    "s_waitcnt lgkmcnt(15)\n\t"
    "v_mfma_f32_32x32x16_bf16 v[32:47], a[32:35], v[128:131], 0\n\t"
    "s_add_u32 s76, s75, 229376\n\t"
    "s_add_u32 m0, s75, 81920\n\t"
    "ds_read_b64_tr_b16 a[96:97], v98 offset:24576\n\t"
    "v_add_f32 v80, v80, v20\n\t"
    "v_exp_f32 v21, v21\n\t"
    "v_mfma_f32_32x32x16_bf16 v[32:47], a[36:39], v[132:135], v[32:47]\n\t"
    "ds_read_b64_tr_b16 a[98:99], v98 offset:26624\n\t"
    "ds_read_b128 a[32:35], v82 offset:8192\n\t"
    "v_cvt_pk_bf16_f32 v50, v20, v21\n\t"
    "v_add_f32 v80, v80, v21\n\t"
    "v_exp_f32 v22, v22\n\t"
    "v_mfma_f32_32x32x16_bf16 v[32:47], a[40:43], v[136:139], v[32:47]\n\t"
    "buffer_load_dwordx4 v110, s[48:51], s76 offen nt lds\n\t"
    "ds_read_b64_tr_b16 a[100:101], v99 offset:24576\n\t"
    "v_add_f32 v80, v80, v22\n\t"
    "v_exp_f32 v23, v23\n\t"
    "v_mfma_f32_32x32x16_bf16 v[32:47], a[44:47], v[140:143], v[32:47]\n\t"
    "ds_read_b64_tr_b16 a[102:103], v99 offset:26624\n\t"
    "ds_read_b128 a[36:39], v83 offset:8192\n\t"
    "v_cvt_pk_bf16_f32 v51, v22, v23\n\t"
    "v_add_f32 v80, v80, v23\n\t"
    "v_exp_f32 v24, v24\n\t"
    "v_mfma_f32_32x32x16_bf16 v[32:47], a[48:51], v[144:147], v[32:47]\n\t"
    "ds_read_b64_tr_b16 a[104:105], v100 offset:24576\n\t"
    "v_add_f32 v80, v80, v24\n\t"
    "v_exp_f32 v25, v25\n\t"
    "v_mfma_f32_32x32x16_bf16 v[32:47], a[52:55], v[148:151], v[32:47]\n\t"
    "ds_read_b64_tr_b16 a[106:107], v100 offset:26624\n\t"
    "ds_read_b128 a[40:43], v84 offset:8192\n\t"
    "v_cvt_pk_bf16_f32 v52, v24, v25\n\t"
    "v_add_f32 v80, v80, v25\n\t"
    "v_exp_f32 v26, v26\n\t"
    "v_mfma_f32_32x32x16_bf16 v[32:47], a[56:59], v[152:155], v[32:47]\n\t"
    "buffer_load_dwordx4 v110, s[48:51], s76 offen offset:1024 nt lds\n\t"
    "ds_read_b64_tr_b16 a[108:109], v101 offset:24576\n\t"
    "v_add_f32 v80, v80, v26\n\t"
    "v_exp_f32 v27, v27\n\t"
    "v_mfma_f32_32x32x16_bf16 v[32:47], a[60:63], v[156:159], v[32:47]\n\t"
    "ds_read_b64_tr_b16 a[110:111], v101 offset:26624\n\t"
    "ds_read_b128 a[44:47], v85 offset:8192\n\t"
    "v_cvt_pk_bf16_f32 v53, v26, v27\n\t"
    "v_add_f32 v80, v80, v27\n\t"
    "v_exp_f32 v28, v28\n\t"
    "s_waitcnt lgkmcnt(12)\n\t"
    "v_mfma_f32_32x32x16_bf16 v[160:175], a[64:67], v[48:51], v[160:175]\n\t"
    "ds_read_b64_tr_b16 a[112:113], v98 offset:28672\n\t"
    "v_add_f32 v80, v80, v28\n\t"
    "v_exp_f32 v29, v29\n\t"
    "v_mfma_f32_32x32x16_bf16 v[176:191], a[68:71], v[48:51], v[176:191]\n\t"
    "ds_read_b64_tr_b16 a[114:115], v98 offset:30720\n\t"
    "ds_read_b128 a[48:51], v86 offset:8192\n\t"
    "v_cvt_pk_bf16_f32 v54, v28, v29\n\t"
    "v_add_f32 v80, v80, v29\n\t"
    "v_exp_f32 v30, v30\n\t"
    "v_mfma_f32_32x32x16_bf16 v[192:207], a[72:75], v[48:51], v[192:207]\n\t"
    "buffer_load_dwordx4 v110, s[48:51], s76 offen offset:2048 nt lds\n\t"
    "ds_read_b64_tr_b16 a[116:117], v99 offset:28672\n\t"
    "v_add_f32 v80, v80, v30\n\t"
    "v_exp_f32 v31, v31\n\t"
    "v_mfma_f32_32x32x16_bf16 v[208:223], a[76:79], v[48:51], v[208:223]\n\t"
    "ds_read_b64_tr_b16 a[118:119], v99 offset:30720\n\t"
    "ds_read_b128 a[52:55], v87 offset:8192\n\t"
    "v_cvt_pk_bf16_f32 v55, v30, v31\n\t"
    "v_add_f32 v80, v80, v31\n\t"
    "v_exp_f32 v32, v32\n\t"
    "v_mfma_f32_32x32x16_bf16 v[160:175], a[80:83], v[52:55], v[160:175]\n\t"
    "ds_read_b64_tr_b16 a[120:121], v100 offset:28672\n\t"
    "v_add_f32 v80, v80, v32\n\t"
    "v_exp_f32 v33, v33\n\t"
    "v_mfma_f32_32x32x16_bf16 v[176:191], a[84:87], v[52:55], v[176:191]\n\t"
    "ds_read_b64_tr_b16 a[122:123], v100 offset:30720\n\t"
    "ds_read_b128 a[56:59], v88 offset:8192\n\t"
    "v_cvt_pk_bf16_f32 v56, v32, v33\n\t"
    "v_add_f32 v80, v80, v33\n\t"
    "v_exp_f32 v34, v34\n\t"
    "v_mfma_f32_32x32x16_bf16 v[192:207], a[88:91], v[52:55], v[192:207]\n\t"
    "buffer_load_dwordx4 v110, s[48:51], s76 offen offset:3072 nt lds\n\t"
    "ds_read_b64_tr_b16 a[124:125], v101 offset:28672\n\t"
    "v_add_f32 v80, v80, v34\n\t"
    "v_exp_f32 v35, v35\n\t"
    "v_mfma_f32_32x32x16_bf16 v[208:223], a[92:95], v[52:55], v[208:223]\n\t"
    "ds_read_b64_tr_b16 a[126:127], v101 offset:30720\n\t"
    "ds_read_b128 a[60:63], v89 offset:8192\n\t"
    "v_cvt_pk_bf16_f32 v57, v34, v35\n\t"
    "v_add_f32 v80, v80, v35\n\t"
    "v_exp_f32 v36, v36\n\t"
    "s_waitcnt vmcnt(8)\n\t"
    "s_barrier\n\t"
    "v_mfma_f32_32x32x16_bf16 v[16:31], a[0:3], v[128:131], 0\n\t"
    "ds_read_b64_tr_b16 a[64:65], v98 offset:49152\n\t"
    "v_add_f32 v80, v80, v36\n\t"
    "v_exp_f32 v37, v37\n\t"
    "v_mfma_f32_32x32x16_bf16 v[16:31], a[4:7], v[132:135], v[16:31]\n\t"
    "ds_read_b128 a[0:3], v82 offset:32768\n\t"
    "ds_read_b64_tr_b16 a[66:67], v98 offset:51200\n\t"
    "v_cvt_pk_bf16_f32 v58, v36, v37\n\t"
    "v_add_f32 v80, v80, v37\n\t"
    "v_exp_f32 v38, v38\n\t"
    "v_mfma_f32_32x32x16_bf16 v[16:31], a[8:11], v[136:139], v[16:31]\n\t"
    "ds_read_b64_tr_b16 a[68:69], v99 offset:49152\n\t"
    "v_add_f32 v80, v80, v38\n\t"
    "v_exp_f32 v39, v39\n\t"
    "s_add_u32 m0, s80, 131072\n\t"
    "s_nop 0\n\t"
    "buffer_load_dwordx4 v70, s[52:55], s74 offen lds\n\t"
    "v_mfma_f32_32x32x16_bf16 v[16:31], a[12:15], v[140:143], v[16:31]\n\t"
    "ds_read_b128 a[4:7], v83 offset:32768\n\t"
    "ds_read_b64_tr_b16 a[70:71], v99 offset:51200\n\t"
    "v_cvt_pk_bf16_f32 v59, v38, v39\n\t"
    "v_add_f32 v80, v80, v39\n\t"
    "v_exp_f32 v40, v40\n\t"
    "v_mfma_f32_32x32x16_bf16 v[16:31], a[16:19], v[144:147], v[16:31]\n\t"
    "ds_read_b64_tr_b16 a[72:73], v100 offset:49152\n\t"
    "v_add_f32 v80, v80, v40\n\t"
    "v_exp_f32 v41, v41\n\t"
    "v_mfma_f32_32x32x16_bf16 v[16:31], a[20:23], v[148:151], v[16:31]\n\t"
    "ds_read_b128 a[8:11], v84 offset:32768\n\t"
    "ds_read_b64_tr_b16 a[74:75], v100 offset:51200\n\t"
    "v_cvt_pk_bf16_f32 v60, v40, v41\n\t"
    "v_add_f32 v80, v80, v41\n\t"
    "v_exp_f32 v42, v42\n\t"
    "v_mfma_f32_32x32x16_bf16 v[16:31], a[24:27], v[152:155], v[16:31]\n\t"
    "ds_read_b64_tr_b16 a[76:77], v101 offset:49152\n\t"
    "v_add_f32 v80, v80, v42\n\t"
    "v_exp_f32 v43, v43\n\t"
    "buffer_load_dwordx4 v71, s[52:55], s74 offen offset:1024 lds\n\t"
    "s_add_u32 s74, s74, 16384\n\t"
    "s_and_b32 s74, s74, 262143\n\t"
    "v_mfma_f32_32x32x16_bf16 v[16:31], a[28:31], v[156:159], v[16:31]\n\t"
    "ds_read_b128 a[12:15], v85 offset:32768\n\t"
    "ds_read_b64_tr_b16 a[78:79], v101 offset:51200\n\t"
    "v_cvt_pk_bf16_f32 v61, v42, v43\n\t"
    "v_add_f32 v80, v80, v43\n\t"
    "v_exp_f32 v44, v44\n\t"
    "s_waitcnt lgkmcnt(13)\n\t"
    "v_mfma_f32_32x32x16_bf16 v[160:175], a[96:99], v[56:59], v[160:175]\n\t"
    "ds_read_b64_tr_b16 a[80:81], v98 offset:53248\n\t"
    "v_add_f32 v80, v80, v44\n\t"
    "v_exp_f32 v45, v45\n\t"
    "v_mfma_f32_32x32x16_bf16 v[176:191], a[100:103], v[56:59], v[176:191]\n\t"
    "ds_read_b128 a[16:19], v86 offset:32768\n\t"
    "ds_read_b64_tr_b16 a[82:83], v98 offset:55296\n\t"
    "v_cvt_pk_bf16_f32 v62, v44, v45\n\t"
    "v_add_f32 v80, v80, v45\n\t"
    "v_exp_f32 v46, v46\n\t"
    "v_mfma_f32_32x32x16_bf16 v[192:207], a[104:107], v[56:59], v[192:207]\n\t"
    "ds_read_b64_tr_b16 a[84:85], v99 offset:53248\n\t"
    "v_add_f32 v80, v80, v46\n\t"
    "v_exp_f32 v47, v47\n\t"
    "s_add_u32 m0, s80, 133120\n\t"
    "s_nop 0\n\t"
    "buffer_load_dwordx4 v70, s[52:55], s74 offen lds\n\t"
    "v_mfma_f32_32x32x16_bf16 v[208:223], a[108:111], v[56:59], v[208:223]\n\t"
    "ds_read_b128 a[20:23], v87 offset:32768\n\t"
    "ds_read_b64_tr_b16 a[86:87], v99 offset:55296\n\t"
    "v_cvt_pk_bf16_f32 v63, v46, v47\n\t"
    "v_add_f32 v80, v80, v47\n\t"
    "v_exp_f32 v16, v16\n\t"
    "v_mfma_f32_32x32x16_bf16 v[160:175], a[112:115], v[60:63], v[160:175]\n\t"
    "ds_read_b64_tr_b16 a[88:89], v100 offset:53248\n\t"
    "v_add_f32 v80, v80, v16\n\t"
    "v_exp_f32 v17, v17\n\t"
    "v_mfma_f32_32x32x16_bf16 v[176:191], a[116:119], v[60:63], v[176:191]\n\t"
    "ds_read_b128 a[24:27], v88 offset:32768\n\t"
    "ds_read_b64_tr_b16 a[90:91], v100 offset:55296\n\t"
    "v_cvt_pk_bf16_f32 v48, v16, v17\n\t"
    "v_add_f32 v80, v80, v17\n\t"
    "v_exp_f32 v18, v18\n\t"
    "v_mfma_f32_32x32x16_bf16 v[192:207], a[120:123], v[60:63], v[192:207]\n\t"
    "ds_read_b64_tr_b16 a[92:93], v101 offset:53248\n\t"
    "v_add_f32 v80, v80, v18\n\t"
    "v_exp_f32 v19, v19\n\t"
    "buffer_load_dwordx4 v71, s[52:55], s74 offen offset:1024 lds\n\t"
    "s_add_u32 s74, s74, 16384\n\t"
    "s_and_b32 s74, s74, 262143\n\t"
    "v_mfma_f32_32x32x16_bf16 v[208:223], a[124:127], v[60:63], v[208:223]\n\t"
    "ds_read_b128 a[28:31], v89 offset:32768\n\t"
    "ds_read_b64_tr_b16 a[94:95], v101 offset:55296\n\t"
    "v_cvt_pk_bf16_f32 v49, v18, v19\n\t"
    "v_add_f32 v80, v80, v19\n\t"
    "v_exp_f32 v20, v20\n\t"
    "s_waitcnt lgkmcnt(15)\n\t"
    "v_mfma_f32_32x32x16_bf16 v[32:47], a[32:35], v[128:131], 0\n\t"
    "s_add_u32 s76, s75, 245760\n\t"
    "s_add_u32 m0, s75, 114688\n\t"
    "ds_read_b64_tr_b16 a[96:97], v98 offset:57344\n\t"
    "v_add_f32 v80, v80, v20\n\t"
    "v_exp_f32 v21, v21\n\t"
    "v_mfma_f32_32x32x16_bf16 v[32:47], a[36:39], v[132:135], v[32:47]\n\t"
    "ds_read_b64_tr_b16 a[98:99], v98 offset:59392\n\t"
    "ds_read_b128 a[32:35], v82 offset:40960\n\t"
    "v_cvt_pk_bf16_f32 v50, v20, v21\n\t"
    "v_add_f32 v80, v80, v21\n\t"
    "v_exp_f32 v22, v22\n\t"
    "v_mfma_f32_32x32x16_bf16 v[32:47], a[40:43], v[136:139], v[32:47]\n\t"
    "buffer_load_dwordx4 v110, s[48:51], s76 offen nt lds\n\t"
    "ds_read_b64_tr_b16 a[100:101], v99 offset:57344\n\t"
    "v_add_f32 v80, v80, v22\n\t"
    "v_exp_f32 v23, v23\n\t"
    "v_mfma_f32_32x32x16_bf16 v[32:47], a[44:47], v[140:143], v[32:47]\n\t"
    "ds_read_b64_tr_b16 a[102:103], v99 offset:59392\n\t"
    "ds_read_b128 a[36:39], v83 offset:40960\n\t"
    "v_cvt_pk_bf16_f32 v51, v22, v23\n\t"
    "v_add_f32 v80, v80, v23\n\t"
    "v_exp_f32 v24, v24\n\t"
    "v_mfma_f32_32x32x16_bf16 v[32:47], a[48:51], v[144:147], v[32:47]\n\t"
    "ds_read_b64_tr_b16 a[104:105], v100 offset:57344\n\t"
    "v_add_f32 v80, v80, v24\n\t"
    "v_exp_f32 v25, v25\n\t"
    "v_mfma_f32_32x32x16_bf16 v[32:47], a[52:55], v[148:151], v[32:47]\n\t"
    "ds_read_b64_tr_b16 a[106:107], v100 offset:59392\n\t"
    "ds_read_b128 a[40:43], v84 offset:40960\n\t"
    "v_cvt_pk_bf16_f32 v52, v24, v25\n\t"
    "v_add_f32 v80, v80, v25\n\t"
    "v_exp_f32 v26, v26\n\t"
    "v_mfma_f32_32x32x16_bf16 v[32:47], a[56:59], v[152:155], v[32:47]\n\t"
    "buffer_load_dwordx4 v110, s[48:51], s76 offen offset:1024 nt lds\n\t"
    "ds_read_b64_tr_b16 a[108:109], v101 offset:57344\n\t"
    "v_add_f32 v80, v80, v26\n\t"
    "v_exp_f32 v27, v27\n\t"
    "v_mfma_f32_32x32x16_bf16 v[32:47], a[60:63], v[156:159], v[32:47]\n\t"
    "ds_read_b64_tr_b16 a[110:111], v101 offset:59392\n\t"
    "ds_read_b128 a[44:47], v85 offset:40960\n\t"
    "v_cvt_pk_bf16_f32 v53, v26, v27\n\t"
    "v_add_f32 v80, v80, v27\n\t"
    "v_exp_f32 v28, v28\n\t"
    "s_waitcnt lgkmcnt(12)\n\t"
    "v_mfma_f32_32x32x16_bf16 v[160:175], a[64:67], v[48:51], v[160:175]\n\t"
    "ds_read_b64_tr_b16 a[112:113], v98 offset:61440\n\t"
    "v_add_f32 v80, v80, v28\n\t"
    "v_exp_f32 v29, v29\n\t"
    "v_mfma_f32_32x32x16_bf16 v[176:191], a[68:71], v[48:51], v[176:191]\n\t"
    "ds_read_b64_tr_b16 a[114:115], v98 offset:63488\n\t"
    "ds_read_b128 a[48:51], v86 offset:40960\n\t"
    "v_cvt_pk_bf16_f32 v54, v28, v29\n\t"
    "v_add_f32 v80, v80, v29\n\t"
    "v_exp_f32 v30, v30\n\t"
    "v_mfma_f32_32x32x16_bf16 v[192:207], a[72:75], v[48:51], v[192:207]\n\t"
    "buffer_load_dwordx4 v110, s[48:51], s76 offen offset:2048 nt lds\n\t"
    "ds_read_b64_tr_b16 a[116:117], v99 offset:61440\n\t"
    "v_add_f32 v80, v80, v30\n\t"
    "v_exp_f32 v31, v31\n\t"
    "v_mfma_f32_32x32x16_bf16 v[208:223], a[76:79], v[48:51], v[208:223]\n\t"
    "ds_read_b64_tr_b16 a[118:119], v99 offset:63488\n\t"
    "ds_read_b128 a[52:55], v87 offset:40960\n\t"
    "v_cvt_pk_bf16_f32 v55, v30, v31\n\t"
    "v_add_f32 v80, v80, v31\n\t"
    "v_exp_f32 v32, v32\n\t"
    "v_mfma_f32_32x32x16_bf16 v[160:175], a[80:83], v[52:55], v[160:175]\n\t"
    "ds_read_b64_tr_b16 a[120:121], v100 offset:61440\n\t"
    "v_add_f32 v80, v80, v32\n\t"
    "v_exp_f32 v33, v33\n\t"
    "v_mfma_f32_32x32x16_bf16 v[176:191], a[84:87], v[52:55], v[176:191]\n\t"
    "ds_read_b64_tr_b16 a[122:123], v100 offset:63488\n\t"
    "ds_read_b128 a[56:59], v88 offset:40960\n\t"
    "v_cvt_pk_bf16_f32 v56, v32, v33\n\t"
    "v_add_f32 v80, v80, v33\n\t"
    "v_exp_f32 v34, v34\n\t"
    "v_mfma_f32_32x32x16_bf16 v[192:207], a[88:91], v[52:55], v[192:207]\n\t"
    "buffer_load_dwordx4 v110, s[48:51], s76 offen offset:3072 nt lds\n\t"
    "ds_read_b64_tr_b16 a[124:125], v101 offset:61440\n\t"
    "v_add_f32 v80, v80, v34\n\t"
    "v_exp_f32 v35, v35\n\t"
    "v_mfma_f32_32x32x16_bf16 v[208:223], a[92:95], v[52:55], v[208:223]\n\t"
    "ds_read_b64_tr_b16 a[126:127], v101 offset:63488\n\t"
    "ds_read_b128 a[60:63], v89 offset:40960\n\t"
    "v_cvt_pk_bf16_f32 v57, v34, v35\n\t"
    "v_add_f32 v80, v80, v35\n\t"
    "v_exp_f32 v36, v36\n\t"
    "s_waitcnt vmcnt(8)\n\t"
    "s_barrier\n\t"
    "v_mfma_f32_32x32x16_bf16 v[16:31], a[0:3], v[128:131], 0\n\t"
    "ds_read_b64_tr_b16 a[64:65], v102 offset:16384\n\t"
    "v_add_f32 v80, v80, v36\n\t"
    "v_exp_f32 v37, v37\n\t"
    "v_mfma_f32_32x32x16_bf16 v[16:31], a[4:7], v[132:135], v[16:31]\n\t"
    "ds_read_b128 a[0:3], v90\n\t"
    "ds_read_b64_tr_b16 a[66:67], v102 offset:18432\n\t"
    "v_cvt_pk_bf16_f32 v58, v36, v37\n\t"
    "v_add_f32 v80, v80, v37\n\t"
    "v_exp_f32 v38, v38\n\t"
    "v_mfma_f32_32x32x16_bf16 v[16:31], a[8:11], v[136:139], v[16:31]\n\t"
    "ds_read_b64_tr_b16 a[68:69], v103 offset:16384\n\t"
    "v_add_f32 v80, v80, v38\n\t"
    "v_exp_f32 v39, v39\n\t"
    "s_add_u32 m0, s80, 135168\n\t"
    "s_nop 0\n\t"
    "buffer_load_dwordx4 v70, s[52:55], s74 offen lds\n\t"
    "v_mfma_f32_32x32x16_bf16 v[16:31], a[12:15], v[140:143], v[16:31]\n\t"
    "ds_read_b128 a[4:7], v91\n\t"
    "ds_read_b64_tr_b16 a[70:71], v103 offset:18432\n\t"
    "v_cvt_pk_bf16_f32 v59, v38, v39\n\t"
    "v_add_f32 v80, v80, v39\n\t"
    "v_exp_f32 v40, v40\n\t"
    "v_mfma_f32_32x32x16_bf16 v[16:31], a[16:19], v[144:147], v[16:31]\n\t"
    "ds_read_b64_tr_b16 a[72:73], v104 offset:16384\n\t"
    "v_add_f32 v80, v80, v40\n\t"
    "v_exp_f32 v41, v41\n\t"
    "v_mfma_f32_32x32x16_bf16 v[16:31], a[20:23], v[148:151], v[16:31]\n\t"
    "ds_read_b128 a[8:11], v92\n\t"
    "ds_read_b64_tr_b16 a[74:75], v104 offset:18432\n\t"
    "v_cvt_pk_bf16_f32 v60, v40, v41\n\t"
    "v_add_f32 v80, v80, v41\n\t"
    "v_exp_f32 v42, v42\n\t"
    "v_mfma_f32_32x32x16_bf16 v[16:31], a[24:27], v[152:155], v[16:31]\n\t"
    "ds_read_b64_tr_b16 a[76:77], v105 offset:16384\n\t"
    "v_add_f32 v80, v80, v42\n\t"
    "v_exp_f32 v43, v43\n\t"
    "buffer_load_dwordx4 v71, s[52:55], s74 offen offset:1024 lds\n\t"
    "s_add_u32 s74, s74, 16384\n\t"
    "s_and_b32 s74, s74, 262143\n\t"
    "v_mfma_f32_32x32x16_bf16 v[16:31], a[28:31], v[156:159], v[16:31]\n\t"
    "ds_read_b128 a[12:15], v93\n\t"
    "ds_read_b64_tr_b16 a[78:79], v105 offset:18432\n\t"
    "v_cvt_pk_bf16_f32 v61, v42, v43\n\t"
    "v_add_f32 v80, v80, v43\n\t"
    "v_exp_f32 v44, v44\n\t"
    "s_waitcnt lgkmcnt(13)\n\t"
    "v_mfma_f32_32x32x16_bf16 v[160:175], a[96:99], v[56:59], v[160:175]\n\t"
    "ds_read_b64_tr_b16 a[80:81], v102 offset:20480\n\t"
    "v_add_f32 v80, v80, v44\n\t"
    "v_exp_f32 v45, v45\n\t"
    "v_mfma_f32_32x32x16_bf16 v[176:191], a[100:103], v[56:59], v[176:191]\n\t"
    "ds_read_b128 a[16:19], v94\n\t"
    "ds_read_b64_tr_b16 a[82:83], v102 offset:22528\n\t"
    "v_cvt_pk_bf16_f32 v62, v44, v45\n\t"
    "v_add_f32 v80, v80, v45\n\t"
    "v_exp_f32 v46, v46\n\t"
    "v_mfma_f32_32x32x16_bf16 v[192:207], a[104:107], v[56:59], v[192:207]\n\t"
    "ds_read_b64_tr_b16 a[84:85], v103 offset:20480\n\t"
    "v_add_f32 v80, v80, v46\n\t"
    "v_exp_f32 v47, v47\n\t"
    "s_add_u32 m0, s80, 137216\n\t"
    "s_nop 0\n\t"
    "buffer_load_dwordx4 v70, s[52:55], s74 offen lds\n\t"
    "v_mfma_f32_32x32x16_bf16 v[208:223], a[108:111], v[56:59], v[208:223]\n\t"
    "ds_read_b128 a[20:23], v95\n\t"
    "ds_read_b64_tr_b16 a[86:87], v103 offset:22528\n\t"
    "v_cvt_pk_bf16_f32 v63, v46, v47\n\t"
    "v_add_f32 v80, v80, v47\n\t"
    "v_exp_f32 v16, v16\n\t"
    "v_mfma_f32_32x32x16_bf16 v[160:175], a[112:115], v[60:63], v[160:175]\n\t"
    "ds_read_b64_tr_b16 a[88:89], v104 offset:20480\n\t"
    "v_add_f32 v80, v80, v16\n\t"
    "v_exp_f32 v17, v17\n\t"
    "v_mfma_f32_32x32x16_bf16 v[176:191], a[116:119], v[60:63], v[176:191]\n\t"
    "ds_read_b128 a[24:27], v96\n\t"
    "ds_read_b64_tr_b16 a[90:91], v104 offset:22528\n\t"
    "v_cvt_pk_bf16_f32 v48, v16, v17\n\t"
    "v_add_f32 v80, v80, v17\n\t"
    "v_exp_f32 v18, v18\n\t"
    "v_mfma_f32_32x32x16_bf16 v[192:207], a[120:123], v[60:63], v[192:207]\n\t"
    "ds_read_b64_tr_b16 a[92:93], v105 offset:20480\n\t"
    "v_add_f32 v80, v80, v18\n\t"
    "v_exp_f32 v19, v19\n\t"
    "buffer_load_dwordx4 v71, s[52:55], s74 offen offset:1024 lds\n\t"
    "s_add_u32 s74, s74, 16384\n\t"
    "s_and_b32 s74, s74, 262143\n\t"
    "v_mfma_f32_32x32x16_bf16 v[208:223], a[124:127], v[60:63], v[208:223]\n\t"
    "ds_read_b128 a[28:31], v97\n\t"
    "ds_read_b64_tr_b16 a[94:95], v105 offset:22528\n\t"
    "v_cvt_pk_bf16_f32 v49, v18, v19\n\t"
    "v_add_f32 v80, v80, v19\n\t"
    "v_exp_f32 v20, v20\n\t"
    "s_waitcnt lgkmcnt(15)\n\t"
    "v_mfma_f32_32x32x16_bf16 v[32:47], a[32:35], v[128:131], 0\n\t"
    "ds_read_b64_tr_b16 a[96:97], v102 offset:24576\n\t"
    "v_add_f32 v80, v80, v20\n\t"
    "v_exp_f32 v21, v21\n\t"
    "v_mfma_f32_32x32x16_bf16 v[32:47], a[36:39], v[132:135], v[32:47]\n\t"
    "ds_read_b64_tr_b16 a[98:99], v102 offset:26624\n\t"
    "ds_read_b128 a[32:35], v90 offset:8192\n\t"
    "v_cvt_pk_bf16_f32 v50, v20, v21\n\t"
    "v_add_f32 v80, v80, v21\n\t"
    "v_exp_f32 v22, v22\n\t"
    "v_mfma_f32_32x32x16_bf16 v[32:47], a[40:43], v[136:139], v[32:47]\n\t"
    "ds_read_b64_tr_b16 a[100:101], v103 offset:24576\n\t"
    "v_add_f32 v80, v80, v22\n\t"
    "v_exp_f32 v23, v23\n\t"
    "s_add_u32 m0, s80, 0\n\t"
    "s_nop 0\n\t"
    "buffer_load_dwordx4 v70, s[52:55], s74 offen lds\n\t"
    "v_mfma_f32_32x32x16_bf16 v[32:47], a[44:47], v[140:143], v[32:47]\n\t"
    "ds_read_b64_tr_b16 a[102:103], v103 offset:26624\n\t"
    "ds_read_b128 a[36:39], v91 offset:8192\n\t"
    "v_cvt_pk_bf16_f32 v51, v22, v23\n\t"
    "v_add_f32 v80, v80, v23\n\t"
    "v_exp_f32 v24, v24\n\t"
    "v_mfma_f32_32x32x16_bf16 v[32:47], a[48:51], v[144:147], v[32:47]\n\t"
    "ds_read_b64_tr_b16 a[104:105], v104 offset:24576\n\t"
    "v_add_f32 v80, v80, v24\n\t"
    "v_exp_f32 v25, v25\n\t"
    "v_mfma_f32_32x32x16_bf16 v[32:47], a[52:55], v[148:151], v[32:47]\n\t"
    "ds_read_b64_tr_b16 a[106:107], v104 offset:26624\n\t"
    "ds_read_b128 a[40:43], v92 offset:8192\n\t"
    "v_cvt_pk_bf16_f32 v52, v24, v25\n\t"
    "v_add_f32 v80, v80, v25\n\t"
    "v_exp_f32 v26, v26\n\t"
    "v_mfma_f32_32x32x16_bf16 v[32:47], a[56:59], v[152:155], v[32:47]\n\t"
    "ds_read_b64_tr_b16 a[108:109], v105 offset:24576\n\t"
    "v_add_f32 v80, v80, v26\n\t"
    "v_exp_f32 v27, v27\n\t"
    "buffer_load_dwordx4 v71, s[52:55], s74 offen offset:1024 lds\n\t"
    "s_add_u32 s74, s74, 16384\n\t"
    "s_and_b32 s74, s74, 262143\n\t"
    "v_mfma_f32_32x32x16_bf16 v[32:47], a[60:63], v[156:159], v[32:47]\n\t"
    "ds_read_b64_tr_b16 a[110:111], v105 offset:26624\n\t"
    "ds_read_b128 a[44:47], v93 offset:8192\n\t"
    "v_cvt_pk_bf16_f32 v53, v26, v27\n\t"
    "v_add_f32 v80, v80, v27\n\t"
    "v_exp_f32 v28, v28\n\t"
    "s_waitcnt lgkmcnt(12)\n\t"
    "v_mfma_f32_32x32x16_bf16 v[160:175], a[64:67], v[48:51], v[160:175]\n\t"
    "ds_read_b64_tr_b16 a[112:113], v102 offset:28672\n\t"
    "v_add_f32 v80, v80, v28\n\t"
    "v_exp_f32 v29, v29\n\t"
    "v_mfma_f32_32x32x16_bf16 v[176:191], a[68:71], v[48:51], v[176:191]\n\t"
    "ds_read_b64_tr_b16 a[114:115], v102 offset:30720\n\t"
    "ds_read_b128 a[48:51], v94 offset:8192\n\t"
    "v_cvt_pk_bf16_f32 v54, v28, v29\n\t"
    "v_add_f32 v80, v80, v29\n\t"
    "v_exp_f32 v30, v30\n\t"
    "v_mfma_f32_32x32x16_bf16 v[192:207], a[72:75], v[48:51], v[192:207]\n\t"
    "ds_read_b64_tr_b16 a[116:117], v103 offset:28672\n\t"
    "v_add_f32 v80, v80, v30\n\t"
    "v_exp_f32 v31, v31\n\t"
    "s_add_u32 m0, s80, 2048\n\t"
    "s_nop 0\n\t"
    "buffer_load_dwordx4 v70, s[52:55], s74 offen lds\n\t"
    "v_mfma_f32_32x32x16_bf16 v[208:223], a[76:79], v[48:51], v[208:223]\n\t"
    "ds_read_b64_tr_b16 a[118:119], v103 offset:30720\n\t"
    "ds_read_b128 a[52:55], v95 offset:8192\n\t"
    "v_cvt_pk_bf16_f32 v55, v30, v31\n\t"
    "v_add_f32 v80, v80, v31\n\t"
    "v_exp_f32 v32, v32\n\t"
    "v_mfma_f32_32x32x16_bf16 v[160:175], a[80:83], v[52:55], v[160:175]\n\t"
    "ds_read_b64_tr_b16 a[120:121], v104 offset:28672\n\t"
    "v_add_f32 v80, v80, v32\n\t"
    "v_exp_f32 v33, v33\n\t"
    "v_mfma_f32_32x32x16_bf16 v[176:191], a[84:87], v[52:55], v[176:191]\n\t"
    "ds_read_b64_tr_b16 a[122:123], v104 offset:30720\n\t"
    "ds_read_b128 a[56:59], v96 offset:8192\n\t"
    "v_cvt_pk_bf16_f32 v56, v32, v33\n\t"
    "v_add_f32 v80, v80, v33\n\t"
    "v_exp_f32 v34, v34\n\t"
    "v_mfma_f32_32x32x16_bf16 v[192:207], a[88:91], v[52:55], v[192:207]\n\t"
    "ds_read_b64_tr_b16 a[124:125], v105 offset:28672\n\t"
    "v_add_f32 v80, v80, v34\n\t"
    "v_exp_f32 v35, v35\n\t"
    "buffer_load_dwordx4 v71, s[52:55], s74 offen offset:1024 lds\n\t"
    "s_add_u32 s74, s74, 16384\n\t"
    "s_and_b32 s74, s74, 262143\n\t"
    "v_mfma_f32_32x32x16_bf16 v[208:223], a[92:95], v[52:55], v[208:223]\n\t"
    "ds_read_b64_tr_b16 a[126:127], v105 offset:30720\n\t"
    "ds_read_b128 a[60:63], v97 offset:8192\n\t"
    "v_cvt_pk_bf16_f32 v57, v34, v35\n\t"
    "v_add_f32 v80, v80, v35\n\t"
    "v_exp_f32 v36, v36\n\t"
    "s_waitcnt vmcnt(8)\n\t"
    "s_barrier\n\t"
    "v_mfma_f32_32x32x16_bf16 v[16:31], a[0:3], v[128:131], 0\n\t"
    "ds_read_b64_tr_b16 a[64:65], v102 offset:49152\n\t"
    "v_add_f32 v80, v80, v36\n\t"
    "v_exp_f32 v37, v37\n\t"
    "v_mfma_f32_32x32x16_bf16 v[16:31], a[4:7], v[132:135], v[16:31]\n\t"
    "ds_read_b64_tr_b16 a[66:67], v102 offset:51200\n\t"
    "v_cvt_pk_bf16_f32 v58, v36, v37\n\t"
    "v_add_f32 v80, v80, v37\n\t"
    "v_exp_f32 v38, v38\n\t"
    "v_mfma_f32_32x32x16_bf16 v[16:31], a[8:11], v[136:139], v[16:31]\n\t"
    "ds_read_b64_tr_b16 a[68:69], v103 offset:49152\n\t"
    "v_add_f32 v80, v80, v38\n\t"
    "v_exp_f32 v39, v39\n\t"
    "s_add_u32 m0, s80, 4096\n\t"
    "s_nop 0\n\t"
    "buffer_load_dwordx4 v70, s[52:55], s74 offen lds\n\t"
    "v_mfma_f32_32x32x16_bf16 v[16:31], a[12:15], v[140:143], v[16:31]\n\t"
    "ds_read_b64_tr_b16 a[70:71], v103 offset:51200\n\t"
    "v_cvt_pk_bf16_f32 v59, v38, v39\n\t"
    "v_add_f32 v80, v80, v39\n\t"
    "v_exp_f32 v40, v40\n\t"
    "v_mfma_f32_32x32x16_bf16 v[16:31], a[16:19], v[144:147], v[16:31]\n\t"
    "ds_read_b64_tr_b16 a[72:73], v104 offset:49152\n\t"
    "v_add_f32 v80, v80, v40\n\t"
    "v_exp_f32 v41, v41\n\t"
    "buffer_load_dwordx4 v71, s[52:55], s74 offen offset:1024 lds\n\t"
    "s_add_u32 s74, s74, 16384\n\t"
    "s_and_b32 s74, s74, 262143\n\t"
    "v_mfma_f32_32x32x16_bf16 v[16:31], a[20:23], v[148:151], v[16:31]\n\t"
    "ds_read_b64_tr_b16 a[74:75], v104 offset:51200\n\t"
    "v_cvt_pk_bf16_f32 v60, v40, v41\n\t"
    "v_add_f32 v80, v80, v41\n\t"
    "v_exp_f32 v42, v42\n\t"
    "v_mfma_f32_32x32x16_bf16 v[16:31], a[24:27], v[152:155], v[16:31]\n\t"
    "ds_read_b64_tr_b16 a[76:77], v105 offset:49152\n\t"
    "v_add_f32 v80, v80, v42\n\t"
    "v_exp_f32 v43, v43\n\t"
    "s_add_u32 m0, s80, 6144\n\t"
    "s_nop 0\n\t"
    "buffer_load_dwordx4 v70, s[52:55], s74 offen lds\n\t"
    "v_mfma_f32_32x32x16_bf16 v[16:31], a[28:31], v[156:159], v[16:31]\n\t"
    "ds_read_b64_tr_b16 a[78:79], v105 offset:51200\n\t"
    "v_cvt_pk_bf16_f32 v61, v42, v43\n\t"
    "v_add_f32 v80, v80, v43\n\t"
    "v_exp_f32 v44, v44\n\t"
    "s_waitcnt lgkmcnt(9)\n\t"
    "v_mfma_f32_32x32x16_bf16 v[160:175], a[96:99], v[56:59], v[160:175]\n\t"
    "ds_read_b64_tr_b16 a[80:81], v102 offset:53248\n\t"
    "v_add_f32 v80, v80, v44\n\t"
    "v_exp_f32 v45, v45\n\t"
    "v_mfma_f32_32x32x16_bf16 v[176:191], a[100:103], v[56:59], v[176:191]\n\t"
    "ds_read_b64_tr_b16 a[82:83], v102 offset:55296\n\t"
    "v_cvt_pk_bf16_f32 v62, v44, v45\n\t"
    "v_add_f32 v80, v80, v45\n\t"
    "v_exp_f32 v46, v46\n\t"
    "v_mfma_f32_32x32x16_bf16 v[192:207], a[104:107], v[56:59], v[192:207]\n\t"
    "ds_read_b64_tr_b16 a[84:85], v103 offset:53248\n\t"
    "v_add_f32 v80, v80, v46\n\t"
    "v_exp_f32 v47, v47\n\t"
    "buffer_load_dwordx4 v71, s[52:55], s74 offen offset:1024 lds\n\t"
    "s_add_u32 s74, s74, 16384\n\t"
    "s_and_b32 s74, s74, 262143\n\t"
    "v_mfma_f32_32x32x16_bf16 v[208:223], a[108:111], v[56:59], v[208:223]\n\t"
    "ds_read_b64_tr_b16 a[86:87], v103 offset:55296\n\t"
    "v_cvt_pk_bf16_f32 v63, v46, v47\n\t"
    "v_add_f32 v80, v80, v47\n\t"
    "v_exp_f32 v16, v16\n\t"
    "v_mfma_f32_32x32x16_bf16 v[160:175], a[112:115], v[60:63], v[160:175]\n\t"
    "ds_read_b64_tr_b16 a[88:89], v104 offset:53248\n\t"
    "v_add_f32 v80, v80, v16\n\t"
    "v_exp_f32 v17, v17\n\t"
    "s_add_u32 m0, s80, 32768\n\t"
    "s_nop 0\n\t"
    "buffer_load_dwordx4 v70, s[52:55], s74 offen lds\n\t"
    "v_mfma_f32_32x32x16_bf16 v[176:191], a[116:119], v[60:63], v[176:191]\n\t"
    "ds_read_b64_tr_b16 a[90:91], v104 offset:55296\n\t"
    "v_cvt_pk_bf16_f32 v48, v16, v17\n\t"
    "v_add_f32 v80, v80, v17\n\t"
    "v_exp_f32 v18, v18\n\t"
    "v_mfma_f32_32x32x16_bf16 v[192:207], a[120:123], v[60:63], v[192:207]\n\t"
    "ds_read_b64_tr_b16 a[92:93], v105 offset:53248\n\t"
    "v_add_f32 v80, v80, v18\n\t"
    "v_exp_f32 v19, v19\n\t"
    "buffer_load_dwordx4 v71, s[52:55], s74 offen offset:1024 lds\n\t"
    "s_add_u32 s74, s74, 16384\n\t"
    "s_and_b32 s74, s74, 262143\n\t"
    "v_mfma_f32_32x32x16_bf16 v[208:223], a[124:127], v[60:63], v[208:223]\n\t"
    "ds_read_b64_tr_b16 a[94:95], v105 offset:55296\n\t"
    "v_cvt_pk_bf16_f32 v49, v18, v19\n\t"
    "v_add_f32 v80, v80, v19\n\t"
    "v_exp_f32 v20, v20\n\t"
    "s_waitcnt lgkmcnt(15)\n\t"
    "v_mfma_f32_32x32x16_bf16 v[32:47], a[32:35], v[128:131], 0\n\t"
    "ds_read_b64_tr_b16 a[96:97], v102 offset:57344\n\t"
    "v_add_f32 v80, v80, v20\n\t"
    "v_exp_f32 v21, v21\n\t"
    "v_mfma_f32_32x32x16_bf16 v[32:47], a[36:39], v[132:135], v[32:47]\n\t"
    "ds_read_b64_tr_b16 a[98:99], v102 offset:59392\n\t"
    "v_cvt_pk_bf16_f32 v50, v20, v21\n\t"
    "v_add_f32 v80, v80, v21\n\t"
    "v_exp_f32 v22, v22\n\t"
    "v_mfma_f32_32x32x16_bf16 v[32:47], a[40:43], v[136:139], v[32:47]\n\t"
    "ds_read_b64_tr_b16 a[100:101], v103 offset:57344\n\t"
    "v_add_f32 v80, v80, v22\n\t"
    "v_exp_f32 v23, v23\n\t"
    "s_add_u32 m0, s80, 34816\n\t"
    "s_nop 0\n\t"
    "buffer_load_dwordx4 v70, s[52:55], s74 offen lds\n\t"
    "v_mfma_f32_32x32x16_bf16 v[32:47], a[44:47], v[140:143], v[32:47]\n\t"
    "ds_read_b64_tr_b16 a[102:103], v103 offset:59392\n\t"
    "v_cvt_pk_bf16_f32 v51, v22, v23\n\t"
    "v_add_f32 v80, v80, v23\n\t"
    "v_exp_f32 v24, v24\n\t"
    "v_mfma_f32_32x32x16_bf16 v[32:47], a[48:51], v[144:147], v[32:47]\n\t"
    "ds_read_b64_tr_b16 a[104:105], v104 offset:57344\n\t"
    "v_add_f32 v80, v80, v24\n\t"
    "v_exp_f32 v25, v25\n\t"
    "buffer_load_dwordx4 v71, s[52:55], s74 offen offset:1024 lds\n\t"
    "s_add_u32 s74, s74, 16384\n\t"
    "s_and_b32 s74, s74, 262143\n\t"
    "v_mfma_f32_32x32x16_bf16 v[32:47], a[52:55], v[148:151], v[32:47]\n\t"
    "ds_read_b64_tr_b16 a[106:107], v104 offset:59392\n\t"
    "v_cvt_pk_bf16_f32 v52, v24, v25\n\t"
    "v_add_f32 v80, v80, v25\n\t"
    "v_exp_f32 v26, v26\n\t"
    "v_mfma_f32_32x32x16_bf16 v[32:47], a[56:59], v[152:155], v[32:47]\n\t"
    "ds_read_b64_tr_b16 a[108:109], v105 offset:57344\n\t"
    "v_add_f32 v80, v80, v26\n\t"
    "v_exp_f32 v27, v27\n\t"
    "s_add_u32 m0, s80, 36864\n\t"
    "s_nop 0\n\t"
    "buffer_load_dwordx4 v70, s[52:55], s74 offen lds\n\t"
    "v_mfma_f32_32x32x16_bf16 v[32:47], a[60:63], v[156:159], v[32:47]\n\t"
    "ds_read_b64_tr_b16 a[110:111], v105 offset:59392\n\t"
    "v_cvt_pk_bf16_f32 v53, v26, v27\n\t"
    "v_add_f32 v80, v80, v27\n\t"
    "v_exp_f32 v28, v28\n\t"
    "s_waitcnt lgkmcnt(8)\n\t"
    "v_mfma_f32_32x32x16_bf16 v[160:175], a[64:67], v[48:51], v[160:175]\n\t"
    "ds_read_b64_tr_b16 a[112:113], v102 offset:61440\n\t"
    "v_add_f32 v80, v80, v28\n\t"
    "v_exp_f32 v29, v29\n\t"
    "v_mfma_f32_32x32x16_bf16 v[176:191], a[68:71], v[48:51], v[176:191]\n\t"
    "ds_read_b64_tr_b16 a[114:115], v102 offset:63488\n\t"
    "v_cvt_pk_bf16_f32 v54, v28, v29\n\t"
    "v_add_f32 v80, v80, v29\n\t"
    "v_exp_f32 v30, v30\n\t"
    "v_mfma_f32_32x32x16_bf16 v[192:207], a[72:75], v[48:51], v[192:207]\n\t"
    "ds_read_b64_tr_b16 a[116:117], v103 offset:61440\n\t"
    "v_add_f32 v80, v80, v30\n\t"
    "v_exp_f32 v31, v31\n\t"
    "buffer_load_dwordx4 v71, s[52:55], s74 offen offset:1024 lds\n\t"
    "s_add_u32 s74, s74, 16384\n\t"
    "s_and_b32 s74, s74, 262143\n\t"
    "v_mfma_f32_32x32x16_bf16 v[208:223], a[76:79], v[48:51], v[208:223]\n\t"
    "ds_read_b64_tr_b16 a[118:119], v103 offset:63488\n\t"
    "v_cvt_pk_bf16_f32 v55, v30, v31\n\t"
    "v_add_f32 v80, v80, v31\n\t"
    "v_exp_f32 v32, v32\n\t"
    "v_mfma_f32_32x32x16_bf16 v[160:175], a[80:83], v[52:55], v[160:175]\n\t"
    "ds_read_b64_tr_b16 a[120:121], v104 offset:61440\n\t"
    "v_add_f32 v80, v80, v32\n\t"
    "v_exp_f32 v33, v33\n\t"
    "s_add_u32 m0, s80, 38912\n\t"
    "s_nop 0\n\t"
    "buffer_load_dwordx4 v70, s[52:55], s74 offen lds\n\t"
    "v_mfma_f32_32x32x16_bf16 v[176:191], a[84:87], v[52:55], v[176:191]\n\t"
    "ds_read_b64_tr_b16 a[122:123], v104 offset:63488\n\t"
    "v_cvt_pk_bf16_f32 v56, v32, v33\n\t"
    "v_add_f32 v80, v80, v33\n\t"
    "v_exp_f32 v34, v34\n\t"
    "v_mfma_f32_32x32x16_bf16 v[192:207], a[88:91], v[52:55], v[192:207]\n\t"
    "ds_read_b64_tr_b16 a[124:125], v105 offset:61440\n\t"
    "v_add_f32 v80, v80, v34\n\t"
    "v_exp_f32 v35, v35\n\t"
    "buffer_load_dwordx4 v71, s[52:55], s74 offen offset:1024 lds\n\t"
    "s_add_u32 s74, s74, 16384\n\t"
    "s_and_b32 s74, s74, 262143\n\t"
    "v_mfma_f32_32x32x16_bf16 v[208:223], a[92:95], v[52:55], v[208:223]\n\t"
    "ds_read_b64_tr_b16 a[126:127], v105 offset:63488\n\t"
    "v_cvt_pk_bf16_f32 v57, v34, v35\n\t"
    "v_add_f32 v80, v80, v35\n\t"
    "v_exp_f32 v36, v36\n\t"
    "s_barrier\n\t"
    "v_add_f32 v80, v80, v36\n\t"
    "v_exp_f32 v37, v37\n\t"
    "s_nop 0\n\t"
    "v_cvt_pk_bf16_f32 v58, v36, v37\n\t"
    "v_add_f32 v80, v80, v37\n\t"
    "v_exp_f32 v38, v38\n\t"
    "s_nop 0\n\t"
    "v_add_f32 v80, v80, v38\n\t"
    "v_exp_f32 v39, v39\n\t"
    "s_nop 0\n\t"
    "v_cvt_pk_bf16_f32 v59, v38, v39\n\t"
    "v_add_f32 v80, v80, v39\n\t"
    "v_exp_f32 v40, v40\n\t"
    "s_nop 0\n\t"
    "v_add_f32 v80, v80, v40\n\t"
    "v_exp_f32 v41, v41\n\t"
    "s_nop 0\n\t"
    "v_cvt_pk_bf16_f32 v60, v40, v41\n\t"
    "v_add_f32 v80, v80, v41\n\t"
    "v_exp_f32 v42, v42\n\t"
    "s_nop 0\n\t"
    "v_add_f32 v80, v80, v42\n\t"
    "v_exp_f32 v43, v43\n\t"
    "s_nop 0\n\t"
    "v_cvt_pk_bf16_f32 v61, v42, v43\n\t"
    "v_add_f32 v80, v80, v43\n\t"
    "v_exp_f32 v44, v44\n\t"
    "s_waitcnt lgkmcnt(0)\n\t"
    "v_mfma_f32_32x32x16_bf16 v[160:175], a[96:99], v[56:59], v[160:175]\n\t"
    "v_add_f32 v80, v80, v44\n\t"
    "v_exp_f32 v45, v45\n\t"
    "v_mfma_f32_32x32x16_bf16 v[176:191], a[100:103], v[56:59], v[176:191]\n\t"
    "v_cvt_pk_bf16_f32 v62, v44, v45\n\t"
    "v_add_f32 v80, v80, v45\n\t"
    "v_exp_f32 v46, v46\n\t"
    "v_mfma_f32_32x32x16_bf16 v[192:207], a[104:107], v[56:59], v[192:207]\n\t"
    "v_add_f32 v80, v80, v46\n\t"
    "v_exp_f32 v47, v47\n\t"
    "v_mfma_f32_32x32x16_bf16 v[208:223], a[108:111], v[56:59], v[208:223]\n\t"
    "v_cvt_pk_bf16_f32 v63, v46, v47\n\t"
    "v_add_f32 v80, v80, v47\n\t"
    "s_nop 0\n\t"
    "v_mfma_f32_32x32x16_bf16 v[160:175], a[112:115], v[60:63], v[160:175]\n\t"
    "v_mfma_f32_32x32x16_bf16 v[176:191], a[116:119], v[60:63], v[176:191]\n\t"
    "v_mfma_f32_32x32x16_bf16 v[192:207], a[120:123], v[60:63], v[192:207]\n\t"
    "v_mfma_f32_32x32x16_bf16 v[208:223], a[124:127], v[60:63], v[208:223]\n\t"
    "LNORM_%=:\n\t"
    "v_mov_b32 v3, v80\n\t"
    "s_nop 1\n\t"
    "v_permlane32_swap_b32 v80, v3\n\t"
    "v_add_f32 v80, v80, v3\n\t"
    "v_rcp_f32 v2, v80\n\t"
    "s_mov_b32 s26, 0x2b800000\n\t"
    "s_mov_b32 s27, 0x53800000\n\t"
    "v_cmp_nle_f32 vcc, s26, v80\n\t"
    "v_cmp_nge_f32 s[98:99], s27, v80\n\t"
    "v_mov_b32 v3, v2\n\t"
    "s_or_b64 vcc, vcc, s[98:99]\n\t"
    "s_cmp_lg_u64 vcc, 0\n\t"
    "s_cselect_b32 s78, 1, 0\n\t"
    "s_andn2_b32 s78, s78, s97\n\t"
    "s_lshl_b32 s26, s21, 2\n\t"
    "s_add_u32 s26, s26, 98304\n\t"
    "v_mov_b32 v7, s26\n\t"
    "v_mov_b32 v8, s78\n\t"
    "ds_write_b32 v7, v8\n\t"
    "v_pk_mul_f32 v[48:49], v[160:161], v[2:3]\n\t"
    "v_pk_mul_f32 v[50:51], v[162:163], v[2:3]\n\t"
    "v_pk_mul_f32 v[52:53], v[164:165], v[2:3]\n\t"
    "v_pk_mul_f32 v[54:55], v[166:167], v[2:3]\n\t"
    "v_pk_mul_f32 v[56:57], v[168:169], v[2:3]\n\t"
    "v_pk_mul_f32 v[58:59], v[170:171], v[2:3]\n\t"
    "v_pk_mul_f32 v[60:61], v[172:173], v[2:3]\n\t"
    "v_pk_mul_f32 v[62:63], v[174:175], v[2:3]\n\t"
    "v_cvt_pk_bf16_f32 v16, v48, v49\n\t"
    "v_cvt_pk_bf16_f32 v17, v50, v51\n\t"
    "v_cvt_pk_bf16_f32 v18, v52, v53\n\t"
    "v_cvt_pk_bf16_f32 v19, v54, v55\n\t"
    "v_cvt_pk_bf16_f32 v20, v56, v57\n\t"
    "v_cvt_pk_bf16_f32 v21, v58, v59\n\t"
    "v_cvt_pk_bf16_f32 v22, v60, v61\n\t"
    "v_cvt_pk_bf16_f32 v23, v62, v63\n\t"
    "ds_write_b128 v72, v[16:19]\n\t"
    "ds_write_b128 v72, v[20:23] offset:2048\n\t"
    "v_pk_mul_f32 v[48:49], v[176:177], v[2:3]\n\t"
    "v_pk_mul_f32 v[50:51], v[178:179], v[2:3]\n\t"
    "v_pk_mul_f32 v[52:53], v[180:181], v[2:3]\n\t"
    "v_pk_mul_f32 v[54:55], v[182:183], v[2:3]\n\t"
    "v_pk_mul_f32 v[56:57], v[184:185], v[2:3]\n\t"
    "v_pk_mul_f32 v[58:59], v[186:187], v[2:3]\n\t"
    "v_pk_mul_f32 v[60:61], v[188:189], v[2:3]\n\t"
    "v_pk_mul_f32 v[62:63], v[190:191], v[2:3]\n\t"
    "v_cvt_pk_bf16_f32 v24, v48, v49\n\t"
    "v_cvt_pk_bf16_f32 v25, v50, v51\n\t"
    "v_cvt_pk_bf16_f32 v26, v52, v53\n\t"
    "v_cvt_pk_bf16_f32 v27, v54, v55\n\t"
    "v_cvt_pk_bf16_f32 v28, v56, v57\n\t"
    "v_cvt_pk_bf16_f32 v29, v58, v59\n\t"
    "v_cvt_pk_bf16_f32 v30, v60, v61\n\t"
    "v_cvt_pk_bf16_f32 v31, v62, v63\n\t"
    "ds_write_b128 v72, v[24:27] offset:4096\n\t"
    "ds_write_b128 v72, v[28:31] offset:6144\n\t"
    "v_pk_mul_f32 v[48:49], v[192:193], v[2:3]\n\t"
    "v_pk_mul_f32 v[50:51], v[194:195], v[2:3]\n\t"
    "v_pk_mul_f32 v[52:53], v[196:197], v[2:3]\n\t"
    "v_pk_mul_f32 v[54:55], v[198:199], v[2:3]\n\t"
    "v_pk_mul_f32 v[56:57], v[200:201], v[2:3]\n\t"
    "v_pk_mul_f32 v[58:59], v[202:203], v[2:3]\n\t"
    "v_pk_mul_f32 v[60:61], v[204:205], v[2:3]\n\t"
    "v_pk_mul_f32 v[62:63], v[206:207], v[2:3]\n\t"
    "v_cvt_pk_bf16_f32 v32, v48, v49\n\t"
    "v_cvt_pk_bf16_f32 v33, v50, v51\n\t"
    "v_cvt_pk_bf16_f32 v34, v52, v53\n\t"
    "v_cvt_pk_bf16_f32 v35, v54, v55\n\t"
    "v_cvt_pk_bf16_f32 v36, v56, v57\n\t"
    "v_cvt_pk_bf16_f32 v37, v58, v59\n\t"
    "v_cvt_pk_bf16_f32 v38, v60, v61\n\t"
    "v_cvt_pk_bf16_f32 v39, v62, v63\n\t"
    "ds_write_b128 v73, v[32:35]\n\t"
    "ds_write_b128 v73, v[36:39] offset:2048\n\t"
    "v_pk_mul_f32 v[48:49], v[208:209], v[2:3]\n\t"
    "v_pk_mul_f32 v[50:51], v[210:211], v[2:3]\n\t"
    "v_pk_mul_f32 v[52:53], v[212:213], v[2:3]\n\t"
    "v_pk_mul_f32 v[54:55], v[214:215], v[2:3]\n\t"
    "v_pk_mul_f32 v[56:57], v[216:217], v[2:3]\n\t"
    "v_pk_mul_f32 v[58:59], v[218:219], v[2:3]\n\t"
    "v_pk_mul_f32 v[60:61], v[220:221], v[2:3]\n\t"
    "v_pk_mul_f32 v[62:63], v[222:223], v[2:3]\n\t"
    "v_cvt_pk_bf16_f32 v40, v48, v49\n\t"
    "v_cvt_pk_bf16_f32 v41, v50, v51\n\t"
    "v_cvt_pk_bf16_f32 v42, v52, v53\n\t"
    "v_cvt_pk_bf16_f32 v43, v54, v55\n\t"
    "v_cvt_pk_bf16_f32 v44, v56, v57\n\t"
    "v_cvt_pk_bf16_f32 v45, v58, v59\n\t"
    "v_cvt_pk_bf16_f32 v46, v60, v61\n\t"
    "v_cvt_pk_bf16_f32 v47, v62, v63\n\t"
    "ds_write_b128 v73, v[40:43] offset:4096\n\t"
    "ds_write_b128 v73, v[44:47] offset:6144\n\t"
    "v_mov_b32 v7, 98304\n\t"
    "s_waitcnt lgkmcnt(0)\n\t"
    "s_barrier\n\t"
    "ds_read_b128 v[8:11], v7\n\t"
    "ds_read_b128 a[128:131], v122\n\t"
    "ds_read_b128 a[132:135], v122 offset:1024\n\t"
    "ds_read_b128 a[136:139], v122 offset:2048\n\t"
    "ds_read_b128 a[140:143], v122 offset:3072\n\t"
    "ds_read_b128 a[144:147], v122 offset:4096\n\t"
    "ds_read_b128 a[148:151], v122 offset:5120\n\t"
    "ds_read_b128 a[152:155], v122 offset:6144\n\t"
    "ds_read_b128 a[156:159], v122 offset:7168\n\t"
    "s_waitcnt lgkmcnt(8)\n\t"
    "v_or3_b32 v8, v8, v9, v10\n\t"
    "v_or_b32 v8, v8, v11\n\t"
    "s_nop 0\n\t"
    "v_readfirstlane_b32 s26, v8\n\t"
    "s_cmp_lg_u32 s26, 0\n\t"
    "s_cbranch_scc1 LSLOW_%=\n\t"
    "s_barrier\n\t"
    "s_lshl_b32 s78, s21, 18\n\t"
    "s_and_b32 s79, s20, 15\n\t"
    "s_lshl_b32 s79, s79, 12\n\t"
    "s_add_u32 s78, s78, s79\n\t"
    "ds_read_b64_tr_b16 a[0:1], v68\n\t"
    "ds_read_b64_tr_b16 a[2:3], v68 offset:1024\n\t"
    "ds_read_b64_tr_b16 a[4:5], v69\n\t"
    "ds_read_b64_tr_b16 a[6:7], v69 offset:1024\n\t"
    "ds_read_b64_tr_b16 a[8:9], v68 offset:2048\n\t"
    "ds_read_b64_tr_b16 a[10:11], v68 offset:3072\n\t"
    "ds_read_b64_tr_b16 a[12:13], v69 offset:2048\n\t"
    "ds_read_b64_tr_b16 a[14:15], v69 offset:3072\n\t"
    "s_waitcnt vmcnt(18)\n\t"
    "ds_read_b64_tr_b16 a[16:17], v68 offset:4096\n\t"
    "ds_read_b64_tr_b16 a[18:19], v68 offset:5120\n\t"
    "ds_read_b64_tr_b16 a[20:21], v69 offset:4096\n\t"
    "ds_read_b64_tr_b16 a[22:23], v69 offset:5120\n\t"
    "ds_read_b128 a[160:163], v122 offset:8192\n\t"
    "ds_read_b128 a[164:167], v122 offset:9216\n\t"
    "s_waitcnt lgkmcnt(10)\n\t"
    "v_mfma_f32_32x32x16_bf16 v[128:143], a[0:3], a[128:131], 0\n\t"
    "s_add_u32 m0, s80, 98304\n\t"
    "s_nop 0\n\t"
    "buffer_load_dwordx4 v70, s[52:55], s74 offen lds\n\t"
    "v_mfma_f32_32x32x16_bf16 v[144:159], a[0:3], a[132:135], 0\n\t"
    "buffer_load_dwordx4 v71, s[52:55], s74 offen offset:1024 lds\n\t"
    "s_add_u32 s74, s74, 16384\n\t"
    "s_and_b32 s74, s74, 262143\n\t"
    "v_mfma_f32_32x32x16_bf16 v[160:175], a[4:7], a[128:131], 0\n\t"
    "s_add_u32 m0, s80, 100352\n\t"
    "s_nop 0\n\t"
    "buffer_load_dwordx4 v70, s[52:55], s74 offen lds\n\t"
    "v_mfma_f32_32x32x16_bf16 v[176:191], a[4:7], a[132:135], 0\n\t"
    "buffer_load_dwordx4 v71, s[52:55], s74 offen offset:1024 lds\n\t"
    "s_add_u32 s74, s74, 16384\n\t"
    "s_and_b32 s74, s74, 262143\n\t"
    "s_waitcnt vmcnt(20)\n\t"
    "ds_read_b64_tr_b16 a[24:25], v68 offset:6144\n\t"
    "ds_read_b64_tr_b16 a[26:27], v68 offset:7168\n\t"
    "ds_read_b64_tr_b16 a[28:29], v69 offset:6144\n\t"
    "ds_read_b64_tr_b16 a[30:31], v69 offset:7168\n\t"
    "ds_read_b128 a[168:171], v122 offset:10240\n\t"
    "ds_read_b128 a[172:175], v122 offset:11264\n\t"
    "s_waitcnt lgkmcnt(12)\n\t"
    "v_mfma_f32_32x32x16_bf16 v[128:143], a[8:11], a[136:139], v[128:143]\n\t"
    "s_add_u32 m0, s80, 102400\n\t"
    "s_nop 0\n\t"
    "buffer_load_dwordx4 v70, s[52:55], s74 offen lds\n\t"
    "v_mfma_f32_32x32x16_bf16 v[144:159], a[8:11], a[140:143], v[144:159]\n\t"
    "buffer_load_dwordx4 v71, s[52:55], s74 offen offset:1024 lds\n\t"
    "s_add_u32 s74, s74, 16384\n\t"
    "s_and_b32 s74, s74, 262143\n\t"
    "v_mfma_f32_32x32x16_bf16 v[160:175], a[12:15], a[136:139], v[160:175]\n\t"
    "s_add_u32 m0, s80, 104448\n\t"
    "s_nop 0\n\t"
    "buffer_load_dwordx4 v70, s[52:55], s74 offen lds\n\t"
    "v_mfma_f32_32x32x16_bf16 v[176:191], a[12:15], a[140:143], v[176:191]\n\t"
    "buffer_load_dwordx4 v71, s[52:55], s74 offen offset:1024 lds\n\t"
    "s_lshl_b32 s74, s84, 16\n\t"
    "s_add_u32 s74, s74, 128\n\t"
    "s_waitcnt vmcnt(22)\n\t"
    "ds_read_b64_tr_b16 a[0:1], v64\n\t"
    "ds_read_b64_tr_b16 a[2:3], v64 offset:1024\n\t"
    "ds_read_b64_tr_b16 a[4:5], v65\n\t"
    "ds_read_b64_tr_b16 a[6:7], v65 offset:1024\n\t"
    "ds_read_b128 a[176:179], v122 offset:12288\n\t"
    "ds_read_b128 a[180:183], v122 offset:13312\n\t"
    "s_waitcnt lgkmcnt(14)\n\t"
    "v_mfma_f32_32x32x16_bf16 v[128:143], a[16:19], a[144:147], v[128:143]\n\t"
    "s_add_u32 m0, s80, 131072\n\t"
    "s_nop 0\n\t"
    "buffer_load_dwordx4 v70, s[52:55], s74 offen lds\n\t"
    "v_mfma_f32_32x32x16_bf16 v[144:159], a[16:19], a[148:151], v[144:159]\n\t"
    "buffer_load_dwordx4 v71, s[52:55], s74 offen offset:1024 lds\n\t"
    "s_add_u32 s74, s74, 16384\n\t"
    "s_and_b32 s74, s74, 262143\n\t"
    "v_mfma_f32_32x32x16_bf16 v[160:175], a[20:23], a[144:147], v[160:175]\n\t"
    "s_add_u32 m0, s80, 133120\n\t"
    "s_nop 0\n\t"
    "buffer_load_dwordx4 v70, s[52:55], s74 offen lds\n\t"
    "v_mfma_f32_32x32x16_bf16 v[176:191], a[20:23], a[148:151], v[176:191]\n\t"
    "s_waitcnt vmcnt(23)\n\t"
    "ds_read_b64_tr_b16 a[8:9], v64 offset:2048\n\t"
    "ds_read_b64_tr_b16 a[10:11], v64 offset:3072\n\t"
    "ds_read_b64_tr_b16 a[12:13], v65 offset:2048\n\t"
    "ds_read_b64_tr_b16 a[14:15], v65 offset:3072\n\t"
    "ds_read_b128 a[184:187], v122 offset:14336\n\t"
    "ds_read_b128 a[188:191], v122 offset:15360\n\t"
    "s_waitcnt lgkmcnt(14)\n\t"
    "v_mfma_f32_32x32x16_bf16 v[128:143], a[24:27], a[152:155], v[128:143]\n\t"
    "buffer_load_dwordx4 v71, s[52:55], s74 offen offset:1024 lds\n\t"
    "s_add_u32 s74, s74, 16384\n\t"
    "s_and_b32 s74, s74, 262143\n\t"
    "v_mfma_f32_32x32x16_bf16 v[144:159], a[24:27], a[156:159], v[144:159]\n\t"
    "v_mfma_f32_32x32x16_bf16 v[160:175], a[28:31], a[152:155], v[160:175]\n\t"
    "s_add_u32 m0, s80, 135168\n\t"
    "s_nop 0\n\t"
    "buffer_load_dwordx4 v70, s[52:55], s74 offen lds\n\t"
    "v_mfma_f32_32x32x16_bf16 v[176:191], a[28:31], a[156:159], v[176:191]\n\t"
    "s_waitcnt vmcnt(23)\n\t"
    "ds_read_b64_tr_b16 a[16:17], v64 offset:4096\n\t"
    "ds_read_b64_tr_b16 a[18:19], v64 offset:5120\n\t"
    "ds_read_b64_tr_b16 a[20:21], v65 offset:4096\n\t"
    "ds_read_b64_tr_b16 a[22:23], v65 offset:5120\n\t"
    "ds_read_b128 a[192:195], v122 offset:16384\n\t"
    "ds_read_b128 a[196:199], v122 offset:17408\n\t"
    "s_waitcnt lgkmcnt(14)\n\t"
    "v_mfma_f32_32x32x16_bf16 v[128:143], a[0:3], a[160:163], v[128:143]\n\t"
    "buffer_load_dwordx4 v71, s[52:55], s74 offen offset:1024 lds\n\t"
    "s_add_u32 s74, s74, 16384\n\t"
    "s_and_b32 s74, s74, 262143\n\t"
    "v_mfma_f32_32x32x16_bf16 v[144:159], a[0:3], a[164:167], v[144:159]\n\t"
    "v_mfma_f32_32x32x16_bf16 v[160:175], a[4:7], a[160:163], v[160:175]\n\t"
    "s_add_u32 m0, s80, 137216\n\t"
    "s_nop 0\n\t"
    "buffer_load_dwordx4 v70, s[52:55], s74 offen lds\n\t"
    "v_mfma_f32_32x32x16_bf16 v[176:191], a[4:7], a[164:167], v[176:191]\n\t"
    "s_waitcnt vmcnt(23)\n\t"
    "ds_read_b64_tr_b16 a[24:25], v64 offset:6144\n\t"
    "ds_read_b64_tr_b16 a[26:27], v64 offset:7168\n\t"
    "ds_read_b64_tr_b16 a[28:29], v65 offset:6144\n\t"
    "ds_read_b64_tr_b16 a[30:31], v65 offset:7168\n\t"
    "ds_read_b128 a[200:203], v122 offset:18432\n\t"
    "ds_read_b128 a[204:207], v122 offset:19456\n\t"
    "s_waitcnt lgkmcnt(14)\n\t"
    "v_mfma_f32_32x32x16_bf16 v[128:143], a[8:11], a[168:171], v[128:143]\n\t"
    "buffer_load_dwordx4 v71, s[52:55], s74 offen offset:1024 lds\n\t"
    "s_add_u32 s74, s74, 16384\n\t"
    "s_and_b32 s74, s74, 262143\n\t"
    "v_mfma_f32_32x32x16_bf16 v[144:159], a[8:11], a[172:175], v[144:159]\n\t"
    "v_mfma_f32_32x32x16_bf16 v[160:175], a[12:15], a[168:171], v[160:175]\n\t"
    "s_add_u32 m0, s80, 0\n\t"
    "s_nop 0\n\t"
    "buffer_load_dwordx4 v70, s[52:55], s74 offen lds\n\t"
    "v_mfma_f32_32x32x16_bf16 v[176:191], a[12:15], a[172:175], v[176:191]\n\t"
    "s_waitcnt vmcnt(23)\n\t"
    "ds_read_b64_tr_b16 a[0:1], v64 offset:32768\n\t"
    "ds_read_b64_tr_b16 a[2:3], v64 offset:33792\n\t"
    "ds_read_b64_tr_b16 a[4:5], v65 offset:32768\n\t"
    "ds_read_b64_tr_b16 a[6:7], v65 offset:33792\n\t"
    "ds_read_b128 a[208:211], v122 offset:20480\n\t"
    "ds_read_b128 a[212:215], v122 offset:21504\n\t"
    "s_waitcnt lgkmcnt(14)\n\t"
    "v_mfma_f32_32x32x16_bf16 v[128:143], a[16:19], a[176:179], v[128:143]\n\t"
    "buffer_load_dwordx4 v71, s[52:55], s74 offen offset:1024 lds\n\t"
    "s_add_u32 s74, s74, 16384\n\t"
    "s_and_b32 s74, s74, 262143\n\t"
    "v_mfma_f32_32x32x16_bf16 v[144:159], a[16:19], a[180:183], v[144:159]\n\t"
    "v_mfma_f32_32x32x16_bf16 v[160:175], a[20:23], a[176:179], v[160:175]\n\t"
    "s_add_u32 m0, s80, 2048\n\t"
    "s_nop 0\n\t"
    "buffer_load_dwordx4 v70, s[52:55], s74 offen lds\n\t"
    "v_mfma_f32_32x32x16_bf16 v[176:191], a[20:23], a[180:183], v[176:191]\n\t"
    "s_waitcnt vmcnt(23)\n\t"
    "ds_read_b64_tr_b16 a[8:9], v64 offset:34816\n\t"
    "ds_read_b64_tr_b16 a[10:11], v64 offset:35840\n\t"
    "ds_read_b64_tr_b16 a[12:13], v65 offset:34816\n\t"
    "ds_read_b64_tr_b16 a[14:15], v65 offset:35840\n\t"
    "ds_read_b128 a[216:219], v122 offset:22528\n\t"
    "ds_read_b128 a[220:223], v122 offset:23552\n\t"
    "s_waitcnt lgkmcnt(14)\n\t"
    "v_mfma_f32_32x32x16_bf16 v[128:143], a[24:27], a[184:187], v[128:143]\n\t"
    "buffer_load_dwordx4 v71, s[52:55], s74 offen offset:1024 lds\n\t"
    "s_add_u32 s74, s74, 16384\n\t"
    "s_and_b32 s74, s74, 262143\n\t"
    "v_mfma_f32_32x32x16_bf16 v[144:159], a[24:27], a[188:191], v[144:159]\n\t"
    "v_mfma_f32_32x32x16_bf16 v[160:175], a[28:31], a[184:187], v[160:175]\n\t"
    "s_add_u32 m0, s80, 4096\n\t"
    "s_nop 0\n\t"
    "buffer_load_dwordx4 v70, s[52:55], s74 offen lds\n\t"
    "v_mfma_f32_32x32x16_bf16 v[176:191], a[28:31], a[188:191], v[176:191]\n\t"
    "s_waitcnt vmcnt(23)\n\t"
    "ds_read_b64_tr_b16 a[16:17], v64 offset:36864\n\t"
    "ds_read_b64_tr_b16 a[18:19], v64 offset:37888\n\t"
    "ds_read_b64_tr_b16 a[20:21], v65 offset:36864\n\t"
    "ds_read_b64_tr_b16 a[22:23], v65 offset:37888\n\t"
    "ds_read_b128 a[224:227], v122 offset:24576\n\t"
    "ds_read_b128 a[228:231], v122 offset:25600\n\t"
    "s_waitcnt lgkmcnt(14)\n\t"
    "v_mfma_f32_32x32x16_bf16 v[128:143], a[0:3], a[192:195], v[128:143]\n\t"
    "buffer_load_dwordx4 v71, s[52:55], s74 offen offset:1024 lds\n\t"
    "s_add_u32 s74, s74, 16384\n\t"
    "s_and_b32 s74, s74, 262143\n\t"
    "v_mfma_f32_32x32x16_bf16 v[144:159], a[0:3], a[196:199], v[144:159]\n\t"
    "v_mfma_f32_32x32x16_bf16 v[160:175], a[4:7], a[192:195], v[160:175]\n\t"
    "s_add_u32 m0, s80, 6144\n\t"
    "s_nop 0\n\t"
    "buffer_load_dwordx4 v70, s[52:55], s74 offen lds\n\t"
    "v_mfma_f32_32x32x16_bf16 v[176:191], a[4:7], a[196:199], v[176:191]\n\t"
    "s_waitcnt vmcnt(23)\n\t"
    "ds_read_b64_tr_b16 a[24:25], v64 offset:38912\n\t"
    "ds_read_b64_tr_b16 a[26:27], v64 offset:39936\n\t"
    "ds_read_b64_tr_b16 a[28:29], v65 offset:38912\n\t"
    "ds_read_b64_tr_b16 a[30:31], v65 offset:39936\n\t"
    "ds_read_b128 a[232:235], v122 offset:26624\n\t"
    "ds_read_b128 a[236:239], v122 offset:27648\n\t"
    "s_waitcnt lgkmcnt(14)\n\t"
    "v_mfma_f32_32x32x16_bf16 v[128:143], a[8:11], a[200:203], v[128:143]\n\t"
    "buffer_load_dwordx4 v71, s[52:55], s74 offen offset:1024 lds\n\t"
    "s_add_u32 s74, s74, 16384\n\t"
    "s_and_b32 s74, s74, 262143\n\t"
    "v_mfma_f32_32x32x16_bf16 v[144:159], a[8:11], a[204:207], v[144:159]\n\t"
    "v_mfma_f32_32x32x16_bf16 v[160:175], a[12:15], a[200:203], v[160:175]\n\t"
    "s_add_u32 m0, s80, 32768\n\t"
    "s_nop 0\n\t"
    "buffer_load_dwordx4 v70, s[52:55], s74 offen lds\n\t"
    "v_mfma_f32_32x32x16_bf16 v[176:191], a[12:15], a[204:207], v[176:191]\n\t"
    "s_waitcnt vmcnt(23)\n\t"
    "ds_read_b64_tr_b16 a[0:1], v66 offset:32768\n\t"
    "ds_read_b64_tr_b16 a[2:3], v66 offset:33792\n\t"
    "ds_read_b64_tr_b16 a[4:5], v67 offset:32768\n\t"
    "ds_read_b64_tr_b16 a[6:7], v67 offset:33792\n\t"
    "ds_read_b128 a[240:243], v122 offset:28672\n\t"
    "ds_read_b128 a[244:247], v122 offset:29696\n\t"
    "s_waitcnt lgkmcnt(14)\n\t"
    "v_mfma_f32_32x32x16_bf16 v[128:143], a[16:19], a[208:211], v[128:143]\n\t"
    "buffer_load_dwordx4 v71, s[52:55], s74 offen offset:1024 lds\n\t"
    "s_add_u32 s74, s74, 16384\n\t"
    "s_and_b32 s74, s74, 262143\n\t"
    "v_mfma_f32_32x32x16_bf16 v[144:159], a[16:19], a[212:215], v[144:159]\n\t"
    "v_mfma_f32_32x32x16_bf16 v[160:175], a[20:23], a[208:211], v[160:175]\n\t"
    "s_add_u32 m0, s80, 34816\n\t"
    "s_nop 0\n\t"
    "buffer_load_dwordx4 v70, s[52:55], s74 offen lds\n\t"
    "v_mfma_f32_32x32x16_bf16 v[176:191], a[20:23], a[212:215], v[176:191]\n\t"
    "s_waitcnt vmcnt(23)\n\t"
    "ds_read_b64_tr_b16 a[8:9], v66 offset:34816\n\t"
    "ds_read_b64_tr_b16 a[10:11], v66 offset:35840\n\t"
    "ds_read_b64_tr_b16 a[12:13], v67 offset:34816\n\t"
    "ds_read_b64_tr_b16 a[14:15], v67 offset:35840\n\t"
    "ds_read_b128 a[248:251], v122 offset:30720\n\t"
    "ds_read_b128 a[252:255], v122 offset:31744\n\t"
    "s_waitcnt lgkmcnt(14)\n\t"
    "v_mfma_f32_32x32x16_bf16 v[128:143], a[24:27], a[216:219], v[128:143]\n\t"
    "buffer_load_dwordx4 v71, s[52:55], s74 offen offset:1024 lds\n\t"
    "s_add_u32 s74, s74, 16384\n\t"
    "s_and_b32 s74, s74, 262143\n\t"
    "v_mfma_f32_32x32x16_bf16 v[144:159], a[24:27], a[220:223], v[144:159]\n\t"
    "v_mfma_f32_32x32x16_bf16 v[160:175], a[28:31], a[216:219], v[160:175]\n\t"
    "s_add_u32 m0, s80, 36864\n\t"
    "s_nop 0\n\t"
    "buffer_load_dwordx4 v70, s[52:55], s74 offen lds\n\t"
    "v_mfma_f32_32x32x16_bf16 v[176:191], a[28:31], a[220:223], v[176:191]\n\t"
    "s_waitcnt vmcnt(23)\n\t"
    "ds_read_b64_tr_b16 a[16:17], v66 offset:36864\n\t"
    "ds_read_b64_tr_b16 a[18:19], v66 offset:37888\n\t"
    "ds_read_b64_tr_b16 a[20:21], v67 offset:36864\n\t"
    "ds_read_b64_tr_b16 a[22:23], v67 offset:37888\n\t"
    "s_waitcnt lgkmcnt(12)\n\t"
    "v_mfma_f32_32x32x16_bf16 v[128:143], a[0:3], a[224:227], v[128:143]\n\t"
    "buffer_load_dwordx4 v71, s[52:55], s74 offen offset:1024 lds\n\t"
    "s_add_u32 s74, s74, 16384\n\t"
    "s_and_b32 s74, s74, 262143\n\t"
    "v_mfma_f32_32x32x16_bf16 v[144:159], a[0:3], a[228:231], v[144:159]\n\t"
    "v_mfma_f32_32x32x16_bf16 v[160:175], a[4:7], a[224:227], v[160:175]\n\t"
    "s_add_u32 m0, s80, 38912\n\t"
    "s_nop 0\n\t"
    "buffer_load_dwordx4 v70, s[52:55], s74 offen lds\n\t"
    "v_mfma_f32_32x32x16_bf16 v[176:191], a[4:7], a[228:231], v[176:191]\n\t"
    "s_waitcnt vmcnt(23)\n\t"
    "ds_read_b64_tr_b16 a[24:25], v66 offset:38912\n\t"
    "ds_read_b64_tr_b16 a[26:27], v66 offset:39936\n\t"
    "ds_read_b64_tr_b16 a[28:29], v67 offset:38912\n\t"
    "ds_read_b64_tr_b16 a[30:31], v67 offset:39936\n\t"
    "s_waitcnt lgkmcnt(10)\n\t"
    "v_mfma_f32_32x32x16_bf16 v[128:143], a[8:11], a[232:235], v[128:143]\n\t"
    "buffer_load_dwordx4 v71, s[52:55], s74 offen offset:1024 lds\n\t"
    "s_add_u32 s74, s74, 16384\n\t"
    "s_and_b32 s74, s74, 262143\n\t"
    "v_mfma_f32_32x32x16_bf16 v[144:159], a[8:11], a[236:239], v[144:159]\n\t"
    "v_mfma_f32_32x32x16_bf16 v[160:175], a[12:15], a[232:235], v[160:175]\n\t"
    "s_add_u32 m0, s80, 98304\n\t"
    "s_nop 0\n\t"
    "buffer_load_dwordx4 v70, s[52:55], s74 offen lds\n\t"
    "v_mfma_f32_32x32x16_bf16 v[176:191], a[12:15], a[236:239], v[176:191]\n\t"
    "s_waitcnt vmcnt(23)\n\t"
    "ds_read_b64_tr_b16 a[0:1], v68\n\t"
    "ds_read_b64_tr_b16 a[2:3], v68 offset:1024\n\t"
    "s_waitcnt vmcnt(21)\n\t"
    "ds_read_b64_tr_b16 a[8:9], v68 offset:2048\n\t"
    "ds_read_b64_tr_b16 a[10:11], v68 offset:3072\n\t"
    "s_waitcnt lgkmcnt(8)\n\t"
    "v_mfma_f32_32x32x16_bf16 v[128:143], a[16:19], a[240:243], v[128:143]\n\t"
    "buffer_load_dwordx4 v71, s[52:55], s74 offen offset:1024 lds\n\t"
    "s_add_u32 s74, s74, 16384\n\t"
    "s_and_b32 s74, s74, 262143\n\t"
    "v_mfma_f32_32x32x16_bf16 v[144:159], a[16:19], a[244:247], v[144:159]\n\t"
    "v_mfma_f32_32x32x16_bf16 v[160:175], a[20:23], a[240:243], v[160:175]\n\t"
    "s_add_u32 m0, s80, 100352\n\t"
    "s_nop 0\n\t"
    "buffer_load_dwordx4 v70, s[52:55], s74 offen lds\n\t"
    "v_mfma_f32_32x32x16_bf16 v[176:191], a[20:23], a[244:247], v[176:191]\n\t"
    "s_waitcnt vmcnt(21)\n\t"
    "ds_read_b64_tr_b16 a[16:17], v68 offset:4096\n\t"
    "ds_read_b64_tr_b16 a[18:19], v68 offset:5120\n\t"
    "s_waitcnt lgkmcnt(6)\n\t"
    "v_mfma_f32_32x32x16_bf16 v[128:143], a[24:27], a[248:251], v[128:143]\n\t"
    "buffer_load_dwordx4 v71, s[52:55], s74 offen offset:1024 lds\n\t"
    "s_add_u32 s74, s74, 16384\n\t"
    "s_and_b32 s74, s74, 262143\n\t"
    "v_mfma_f32_32x32x16_bf16 v[144:159], a[24:27], a[252:255], v[144:159]\n\t"
    "v_mfma_f32_32x32x16_bf16 v[160:175], a[28:31], a[248:251], v[160:175]\n\t"
    "s_add_u32 m0, s80, 102400\n\t"
    "s_nop 0\n\t"
    "buffer_load_dwordx4 v70, s[52:55], s74 offen lds\n\t"
    "v_mfma_f32_32x32x16_bf16 v[176:191], a[28:31], a[252:255], v[176:191]\n\t"
    "s_waitcnt vmcnt(21)\n\t"
    "ds_read_b64_tr_b16 a[24:25], v68 offset:6144\n\t"
    "ds_read_b64_tr_b16 a[26:27], v68 offset:7168\n\t"
    "s_waitcnt lgkmcnt(6)\n\t"
    "v_mfma_f32_32x32x16_bf16 v[192:207], a[0:3], a[128:131], 0\n\t"
    "buffer_load_dwordx4 v71, s[52:55], s74 offen offset:1024 lds\n\t"
    "s_add_u32 s74, s74, 16384\n\t"
    "s_and_b32 s74, s74, 262143\n\t"
    "v_mfma_f32_32x32x16_bf16 v[208:223], a[0:3], a[132:135], 0\n\t"
    "s_waitcnt vmcnt(20)\n\t"
    "ds_read_b64_tr_b16 a[0:1], v64\n\t"
    "ds_read_b64_tr_b16 a[2:3], v64 offset:1024\n\t"
    "s_waitcnt lgkmcnt(6)\n\t"
    "v_mfma_f32_32x32x16_bf16 v[192:207], a[8:11], a[136:139], v[192:207]\n\t"
    "s_add_u32 m0, s80, 104448\n\t"
    "s_nop 0\n\t"
    "buffer_load_dwordx4 v70, s[52:55], s74 offen lds\n\t"
    "v_mfma_f32_32x32x16_bf16 v[208:223], a[8:11], a[140:143], v[208:223]\n\t"
    "v_cvt_pk_bf16_f32 v128, v128, v129\n\t"
    "v_cvt_pk_bf16_f32 v129, v130, v131\n\t"
    "v_cvt_pk_bf16_f32 v130, v132, v133\n\t"
    "v_cvt_pk_bf16_f32 v131, v134, v135\n\t"
    "s_add_u32 s27, s78, 0\n\t"
    "buffer_store_dwordx4 v[128:131], v120, s[56:59], s27 offen sc1\n\t"
    "s_waitcnt vmcnt(20)\n\t"
    "ds_read_b64_tr_b16 a[8:9], v64 offset:2048\n\t"
    "ds_read_b64_tr_b16 a[10:11], v64 offset:3072\n\t"
    "s_waitcnt lgkmcnt(6)\n\t"
    "v_mfma_f32_32x32x16_bf16 v[192:207], a[16:19], a[144:147], v[192:207]\n\t"
    "buffer_load_dwordx4 v71, s[52:55], s74 offen offset:1024 lds\n\t"
    "s_add_u32 s74, s74, 16384\n\t"
    "s_and_b32 s74, s74, 262143\n\t"
    "v_mfma_f32_32x32x16_bf16 v[208:223], a[16:19], a[148:151], v[208:223]\n\t"
    "s_waitcnt vmcnt(19)\n\t"
    "ds_read_b64_tr_b16 a[16:17], v64 offset:4096\n\t"
    "ds_read_b64_tr_b16 a[18:19], v64 offset:5120\n\t"
    "s_waitcnt lgkmcnt(6)\n\t"
    "v_mfma_f32_32x32x16_bf16 v[192:207], a[24:27], a[152:155], v[192:207]\n\t"
    "v_mfma_f32_32x32x16_bf16 v[208:223], a[24:27], a[156:159], v[208:223]\n\t"
    "v_cvt_pk_bf16_f32 v136, v136, v137\n\t"
    "v_cvt_pk_bf16_f32 v137, v138, v139\n\t"
    "v_cvt_pk_bf16_f32 v138, v140, v141\n\t"
    "v_cvt_pk_bf16_f32 v139, v142, v143\n\t"
    "s_add_u32 s27, s78, 1024\n\t"
    "buffer_store_dwordx4 v[136:139], v120, s[56:59], s27 offen sc1\n\t"
    "s_waitcnt vmcnt(18)\n\t"
    "ds_read_b64_tr_b16 a[24:25], v64 offset:6144\n\t"
    "ds_read_b64_tr_b16 a[26:27], v64 offset:7168\n\t"
    "s_waitcnt lgkmcnt(6)\n\t"
    "v_mfma_f32_32x32x16_bf16 v[192:207], a[0:3], a[160:163], v[192:207]\n\t"
    "v_mfma_f32_32x32x16_bf16 v[208:223], a[0:3], a[164:167], v[208:223]\n\t"
    "s_waitcnt vmcnt(16)\n\t"
    "ds_read_b64_tr_b16 a[0:1], v64 offset:32768\n\t"
    "ds_read_b64_tr_b16 a[2:3], v64 offset:33792\n\t"
    "s_waitcnt lgkmcnt(6)\n\t"
    "v_mfma_f32_32x32x16_bf16 v[192:207], a[8:11], a[168:171], v[192:207]\n\t"
    "v_mfma_f32_32x32x16_bf16 v[208:223], a[8:11], a[172:175], v[208:223]\n\t"
    "v_cvt_pk_bf16_f32 v144, v144, v145\n\t"
    "v_cvt_pk_bf16_f32 v145, v146, v147\n\t"
    "v_cvt_pk_bf16_f32 v146, v148, v149\n\t"
    "v_cvt_pk_bf16_f32 v147, v150, v151\n\t"
    "s_add_u32 s27, s78, 2048\n\t"
    "buffer_store_dwordx4 v[144:147], v120, s[56:59], s27 offen sc1\n\t"
    "s_waitcnt vmcnt(15)\n\t"
    "ds_read_b64_tr_b16 a[8:9], v64 offset:34816\n\t"
    "ds_read_b64_tr_b16 a[10:11], v64 offset:35840\n\t"
    "s_waitcnt lgkmcnt(6)\n\t"
    "v_mfma_f32_32x32x16_bf16 v[192:207], a[16:19], a[176:179], v[192:207]\n\t"
    "v_mfma_f32_32x32x16_bf16 v[208:223], a[16:19], a[180:183], v[208:223]\n\t"
    "s_waitcnt vmcnt(13)\n\t"
    "ds_read_b64_tr_b16 a[16:17], v64 offset:36864\n\t"
    "ds_read_b64_tr_b16 a[18:19], v64 offset:37888\n\t"
    "s_waitcnt lgkmcnt(6)\n\t"
    "v_mfma_f32_32x32x16_bf16 v[192:207], a[24:27], a[184:187], v[192:207]\n\t"
    "v_mfma_f32_32x32x16_bf16 v[208:223], a[24:27], a[188:191], v[208:223]\n\t"
    "v_cvt_pk_bf16_f32 v152, v152, v153\n\t"
    "v_cvt_pk_bf16_f32 v153, v154, v155\n\t"
    "v_cvt_pk_bf16_f32 v154, v156, v157\n\t"
    "v_cvt_pk_bf16_f32 v155, v158, v159\n\t"
    "s_add_u32 s27, s78, 3072\n\t"
    "buffer_store_dwordx4 v[152:155], v120, s[56:59], s27 offen sc1\n\t"
    "s_waitcnt vmcnt(12)\n\t"
    "ds_read_b64_tr_b16 a[24:25], v64 offset:38912\n\t"
    "ds_read_b64_tr_b16 a[26:27], v64 offset:39936\n\t"
    "s_waitcnt lgkmcnt(6)\n\t"
    "v_mfma_f32_32x32x16_bf16 v[192:207], a[0:3], a[192:195], v[192:207]\n\t"
    "v_mfma_f32_32x32x16_bf16 v[208:223], a[0:3], a[196:199], v[208:223]\n\t"
    "s_waitcnt vmcnt(10)\n\t"
    "ds_read_b64_tr_b16 a[0:1], v66 offset:32768\n\t"
    "ds_read_b64_tr_b16 a[2:3], v66 offset:33792\n\t"
    "s_waitcnt lgkmcnt(6)\n\t"
    "v_mfma_f32_32x32x16_bf16 v[192:207], a[8:11], a[200:203], v[192:207]\n\t"
    "v_mfma_f32_32x32x16_bf16 v[208:223], a[8:11], a[204:207], v[208:223]\n\t"
    "v_cvt_pk_bf16_f32 v160, v160, v161\n\t"
    "v_cvt_pk_bf16_f32 v161, v162, v163\n\t"
    "v_cvt_pk_bf16_f32 v162, v164, v165\n\t"
    "v_cvt_pk_bf16_f32 v163, v166, v167\n\t"
    "s_add_u32 s27, s78, 65536\n\t"
    "buffer_store_dwordx4 v[160:163], v120, s[56:59], s27 offen sc1\n\t"
    "s_waitcnt vmcnt(9)\n\t"
    "ds_read_b64_tr_b16 a[8:9], v66 offset:34816\n\t"
    "ds_read_b64_tr_b16 a[10:11], v66 offset:35840\n\t"
    "s_waitcnt lgkmcnt(6)\n\t"
    "v_mfma_f32_32x32x16_bf16 v[192:207], a[16:19], a[208:211], v[192:207]\n\t"
    "v_mfma_f32_32x32x16_bf16 v[208:223], a[16:19], a[212:215], v[208:223]\n\t"
    "s_waitcnt vmcnt(7)\n\t"
    "ds_read_b64_tr_b16 a[16:17], v66 offset:36864\n\t"
    "ds_read_b64_tr_b16 a[18:19], v66 offset:37888\n\t"
    "s_waitcnt lgkmcnt(6)\n\t"
    "v_mfma_f32_32x32x16_bf16 v[192:207], a[24:27], a[216:219], v[192:207]\n\t"
    "v_mfma_f32_32x32x16_bf16 v[208:223], a[24:27], a[220:223], v[208:223]\n\t"
    "v_cvt_pk_bf16_f32 v168, v168, v169\n\t"
    "v_cvt_pk_bf16_f32 v169, v170, v171\n\t"
    "v_cvt_pk_bf16_f32 v170, v172, v173\n\t"
    "v_cvt_pk_bf16_f32 v171, v174, v175\n\t"
    "s_add_u32 s27, s78, 66560\n\t"
    "buffer_store_dwordx4 v[168:171], v120, s[56:59], s27 offen sc1\n\t"
    "s_waitcnt vmcnt(5)\n\t"
    "ds_read_b64_tr_b16 a[24:25], v66 offset:38912\n\t"
    "ds_read_b64_tr_b16 a[26:27], v66 offset:39936\n\t"
    "s_waitcnt lgkmcnt(6)\n\t"
    "v_mfma_f32_32x32x16_bf16 v[192:207], a[0:3], a[224:227], v[192:207]\n\t"
    "v_mfma_f32_32x32x16_bf16 v[208:223], a[0:3], a[228:231], v[208:223]\n\t"
    "ds_read_b64_tr_b16 a[4:5], v69\n\t"
    "ds_read_b64_tr_b16 a[6:7], v69 offset:1024\n\t"
    "s_waitcnt lgkmcnt(6)\n\t"
    "v_mfma_f32_32x32x16_bf16 v[192:207], a[8:11], a[232:235], v[192:207]\n\t"
    "v_mfma_f32_32x32x16_bf16 v[208:223], a[8:11], a[236:239], v[208:223]\n\t"
    "v_cvt_pk_bf16_f32 v176, v176, v177\n\t"
    "v_cvt_pk_bf16_f32 v177, v178, v179\n\t"
    "v_cvt_pk_bf16_f32 v178, v180, v181\n\t"
    "v_cvt_pk_bf16_f32 v179, v182, v183\n\t"
    "s_add_u32 s27, s78, 67584\n\t"
    "buffer_store_dwordx4 v[176:179], v120, s[56:59], s27 offen sc1\n\t"
    "ds_read_b64_tr_b16 a[12:13], v69 offset:2048\n\t"
    "ds_read_b64_tr_b16 a[14:15], v69 offset:3072\n\t"
    "s_waitcnt lgkmcnt(6)\n\t"
    "v_mfma_f32_32x32x16_bf16 v[192:207], a[16:19], a[240:243], v[192:207]\n\t"
    "v_mfma_f32_32x32x16_bf16 v[208:223], a[16:19], a[244:247], v[208:223]\n\t"
    "ds_read_b64_tr_b16 a[20:21], v69 offset:4096\n\t"
    "ds_read_b64_tr_b16 a[22:23], v69 offset:5120\n\t"
    "s_waitcnt lgkmcnt(6)\n\t"
    "v_mfma_f32_32x32x16_bf16 v[192:207], a[24:27], a[248:251], v[192:207]\n\t"
    "v_mfma_f32_32x32x16_bf16 v[208:223], a[24:27], a[252:255], v[208:223]\n\t"
    "v_cvt_pk_bf16_f32 v184, v184, v185\n\t"
    "v_cvt_pk_bf16_f32 v185, v186, v187\n\t"
    "v_cvt_pk_bf16_f32 v186, v188, v189\n\t"
    "v_cvt_pk_bf16_f32 v187, v190, v191\n\t"
    "s_add_u32 s27, s78, 68608\n\t"
    "buffer_store_dwordx4 v[184:187], v120, s[56:59], s27 offen sc1\n\t"
    "ds_read_b64_tr_b16 a[28:29], v69 offset:6144\n\t"
    "ds_read_b64_tr_b16 a[30:31], v69 offset:7168\n\t"
    "s_waitcnt lgkmcnt(6)\n\t"
    "v_mfma_f32_32x32x16_bf16 v[224:239], a[4:7], a[128:131], 0\n\t"
    "v_mfma_f32_32x32x16_bf16 v[240:255], a[4:7], a[132:135], 0\n\t"
    "ds_read_b64_tr_b16 a[4:5], v65\n\t"
    "ds_read_b64_tr_b16 a[6:7], v65 offset:1024\n\t"
    "s_waitcnt lgkmcnt(6)\n\t"
    "v_mfma_f32_32x32x16_bf16 v[224:239], a[12:15], a[136:139], v[224:239]\n\t"
    "v_mfma_f32_32x32x16_bf16 v[240:255], a[12:15], a[140:143], v[240:255]\n\t"
    "v_cvt_pk_bf16_f32 v192, v192, v193\n\t"
    "v_cvt_pk_bf16_f32 v193, v194, v195\n\t"
    "v_cvt_pk_bf16_f32 v194, v196, v197\n\t"
    "v_cvt_pk_bf16_f32 v195, v198, v199\n\t"
    "s_add_u32 s27, s78, 131072\n\t"
    "buffer_store_dwordx4 v[192:195], v120, s[56:59], s27 offen sc1\n\t"
    "ds_read_b64_tr_b16 a[12:13], v65 offset:2048\n\t"
    "ds_read_b64_tr_b16 a[14:15], v65 offset:3072\n\t"
    "s_waitcnt lgkmcnt(6)\n\t"
    "v_mfma_f32_32x32x16_bf16 v[224:239], a[20:23], a[144:147], v[224:239]\n\t"
    "v_mfma_f32_32x32x16_bf16 v[240:255], a[20:23], a[148:151], v[240:255]\n\t"
    "ds_read_b64_tr_b16 a[20:21], v65 offset:4096\n\t"
    "ds_read_b64_tr_b16 a[22:23], v65 offset:5120\n\t"
    "s_waitcnt lgkmcnt(6)\n\t"
    "v_mfma_f32_32x32x16_bf16 v[224:239], a[28:31], a[152:155], v[224:239]\n\t"
    "v_mfma_f32_32x32x16_bf16 v[240:255], a[28:31], a[156:159], v[240:255]\n\t"
    "v_cvt_pk_bf16_f32 v200, v200, v201\n\t"
    "v_cvt_pk_bf16_f32 v201, v202, v203\n\t"
    "v_cvt_pk_bf16_f32 v202, v204, v205\n\t"
    "v_cvt_pk_bf16_f32 v203, v206, v207\n\t"
    "s_add_u32 s27, s78, 132096\n\t"
    "buffer_store_dwordx4 v[200:203], v120, s[56:59], s27 offen sc1\n\t"
    "ds_read_b64_tr_b16 a[28:29], v65 offset:6144\n\t"
    "ds_read_b64_tr_b16 a[30:31], v65 offset:7168\n\t"
    "s_waitcnt lgkmcnt(6)\n\t"
    "v_mfma_f32_32x32x16_bf16 v[224:239], a[4:7], a[160:163], v[224:239]\n\t"
    "v_mfma_f32_32x32x16_bf16 v[240:255], a[4:7], a[164:167], v[240:255]\n\t"
    "ds_read_b64_tr_b16 a[4:5], v65 offset:32768\n\t"
    "ds_read_b64_tr_b16 a[6:7], v65 offset:33792\n\t"
    "s_waitcnt lgkmcnt(6)\n\t"
    "v_mfma_f32_32x32x16_bf16 v[224:239], a[12:15], a[168:171], v[224:239]\n\t"
    "v_mfma_f32_32x32x16_bf16 v[240:255], a[12:15], a[172:175], v[240:255]\n\t"
    "v_cvt_pk_bf16_f32 v208, v208, v209\n\t"
    "v_cvt_pk_bf16_f32 v209, v210, v211\n\t"
    "v_cvt_pk_bf16_f32 v210, v212, v213\n\t"
    "v_cvt_pk_bf16_f32 v211, v214, v215\n\t"
    "s_add_u32 s27, s78, 133120\n\t"
    "buffer_store_dwordx4 v[208:211], v120, s[56:59], s27 offen sc1\n\t"
    "ds_read_b64_tr_b16 a[12:13], v65 offset:34816\n\t"
    "ds_read_b64_tr_b16 a[14:15], v65 offset:35840\n\t"
    "s_waitcnt lgkmcnt(6)\n\t"
    "v_mfma_f32_32x32x16_bf16 v[224:239], a[20:23], a[176:179], v[224:239]\n\t"
    "v_mfma_f32_32x32x16_bf16 v[240:255], a[20:23], a[180:183], v[240:255]\n\t"
    "ds_read_b64_tr_b16 a[20:21], v65 offset:36864\n\t"
    "ds_read_b64_tr_b16 a[22:23], v65 offset:37888\n\t"
    "s_waitcnt lgkmcnt(6)\n\t"
    "v_mfma_f32_32x32x16_bf16 v[224:239], a[28:31], a[184:187], v[224:239]\n\t"
    "v_mfma_f32_32x32x16_bf16 v[240:255], a[28:31], a[188:191], v[240:255]\n\t"
    "v_cvt_pk_bf16_f32 v216, v216, v217\n\t"
    "v_cvt_pk_bf16_f32 v217, v218, v219\n\t"
    "v_cvt_pk_bf16_f32 v218, v220, v221\n\t"
    "v_cvt_pk_bf16_f32 v219, v222, v223\n\t"
    "s_add_u32 s27, s78, 134144\n\t"
    "buffer_store_dwordx4 v[216:219], v120, s[56:59], s27 offen sc1\n\t"
    "ds_read_b64_tr_b16 a[28:29], v65 offset:38912\n\t"
    "ds_read_b64_tr_b16 a[30:31], v65 offset:39936\n\t"
    "s_waitcnt lgkmcnt(6)\n\t"
    "v_mfma_f32_32x32x16_bf16 v[224:239], a[4:7], a[192:195], v[224:239]\n\t"
    "v_mfma_f32_32x32x16_bf16 v[240:255], a[4:7], a[196:199], v[240:255]\n\t"
    "ds_read_b64_tr_b16 a[4:5], v67 offset:32768\n\t"
    "ds_read_b64_tr_b16 a[6:7], v67 offset:33792\n\t"
    "s_waitcnt lgkmcnt(6)\n\t"
    "v_mfma_f32_32x32x16_bf16 v[224:239], a[12:15], a[200:203], v[224:239]\n\t"
    "v_mfma_f32_32x32x16_bf16 v[240:255], a[12:15], a[204:207], v[240:255]\n\t"
    "ds_read_b64_tr_b16 a[12:13], v67 offset:34816\n\t"
    "ds_read_b64_tr_b16 a[14:15], v67 offset:35840\n\t"
    "s_waitcnt lgkmcnt(6)\n\t"
    "v_mfma_f32_32x32x16_bf16 v[224:239], a[20:23], a[208:211], v[224:239]\n\t"
    "v_mfma_f32_32x32x16_bf16 v[240:255], a[20:23], a[212:215], v[240:255]\n\t"
    "ds_read_b64_tr_b16 a[20:21], v67 offset:36864\n\t"
    "ds_read_b64_tr_b16 a[22:23], v67 offset:37888\n\t"
    "s_waitcnt lgkmcnt(6)\n\t"
    "v_mfma_f32_32x32x16_bf16 v[224:239], a[28:31], a[216:219], v[224:239]\n\t"
    "v_mfma_f32_32x32x16_bf16 v[240:255], a[28:31], a[220:223], v[240:255]\n\t"
    "ds_read_b64_tr_b16 a[28:29], v67 offset:38912\n\t"
    "ds_read_b64_tr_b16 a[30:31], v67 offset:39936\n\t"
    "s_waitcnt lgkmcnt(6)\n\t"
    "v_mfma_f32_32x32x16_bf16 v[224:239], a[4:7], a[224:227], v[224:239]\n\t"
    "v_mfma_f32_32x32x16_bf16 v[240:255], a[4:7], a[228:231], v[240:255]\n\t"
    "s_waitcnt lgkmcnt(4)\n\t"
    "v_mfma_f32_32x32x16_bf16 v[224:239], a[12:15], a[232:235], v[224:239]\n\t"
    "v_mfma_f32_32x32x16_bf16 v[240:255], a[12:15], a[236:239], v[240:255]\n\t"
    "s_waitcnt lgkmcnt(2)\n\t"
    "v_mfma_f32_32x32x16_bf16 v[224:239], a[20:23], a[240:243], v[224:239]\n\t"
    "v_mfma_f32_32x32x16_bf16 v[240:255], a[20:23], a[244:247], v[240:255]\n\t"
    "s_waitcnt lgkmcnt(0)\n\t"
    "v_mfma_f32_32x32x16_bf16 v[224:239], a[28:31], a[248:251], v[224:239]\n\t"
    "v_mfma_f32_32x32x16_bf16 v[240:255], a[28:31], a[252:255], v[240:255]\n\t"
    "s_nop 10\n\t"
    "v_cvt_pk_bf16_f32 v224, v224, v225\n\t"
    "s_nop 9\n\t"
    "v_cvt_pk_bf16_f32 v225, v226, v227\n\t"
    "s_nop 8\n\t"
    "v_cvt_pk_bf16_f32 v226, v228, v229\n\t"
    "s_nop 7\n\t"
    "v_cvt_pk_bf16_f32 v227, v230, v231\n\t"
    "s_add_u32 s27, s78, 196608\n\t"
    "buffer_store_dwordx4 v[224:227], v120, s[56:59], s27 offen sc1\n\t"
    "s_nop 4\n\t"
    "v_cvt_pk_bf16_f32 v232, v232, v233\n\t"
    "s_nop 3\n\t"
    "v_cvt_pk_bf16_f32 v233, v234, v235\n\t"
    "s_nop 2\n\t"
    "v_cvt_pk_bf16_f32 v234, v236, v237\n\t"
    "s_nop 1\n\t"
    "v_cvt_pk_bf16_f32 v235, v238, v239\n\t"
    "s_add_u32 s27, s78, 197632\n\t"
    "buffer_store_dwordx4 v[232:235], v120, s[56:59], s27 offen sc1\n\t"
    "v_cvt_pk_bf16_f32 v240, v240, v241\n\t"
    "v_cvt_pk_bf16_f32 v241, v242, v243\n\t"
    "v_cvt_pk_bf16_f32 v242, v244, v245\n\t"
    "v_cvt_pk_bf16_f32 v243, v246, v247\n\t"
    "s_add_u32 s27, s78, 198656\n\t"
    "buffer_store_dwordx4 v[240:243], v120, s[56:59], s27 offen sc1\n\t"
    "v_cvt_pk_bf16_f32 v248, v248, v249\n\t"
    "v_cvt_pk_bf16_f32 v249, v250, v251\n\t"
    "v_cvt_pk_bf16_f32 v250, v252, v253\n\t"
    "v_cvt_pk_bf16_f32 v251, v254, v255\n\t"
    "s_add_u32 s27, s78, 199680\n\t"
    "buffer_store_dwordx4 v[248:251], v120, s[56:59], s27 offen sc1\n\t"
    "v_mov_b32 v10, 0\n\t"
    "s_cmp_lg_u32 s21, 0\n\t"
    "s_cbranch_scc1 LNZ_%=\n\t"
    "ds_write_b32 v10, v10\n\t"
    "LNZ_%=:\n\t"
    "s_waitcnt vmcnt(0) lgkmcnt(0)\n\t"
    "s_barrier\n\t"
    "v_mov_b32 v2, s94\n\t"
    "v_mov_b32 v3, s95\n\t"
    "s_and_b32 s78, s20, 15\n\t"
    "s_lshl_b32 s28, 1, s78\n\t"
    "s_lshl_b32 s78, s78, 16\n\t"
    "s_lshl_b32 s79, s21, 10\n\t"
    "s_add_u32 s78, s78, s79\n\t"
    "s_mov_b32 s96, 0\n\t"
    "s_mov_b32 s29, 0\n\t"
    "s_cmp_lg_u32 s21, 0\n\t"
    "s_cbranch_scc1 LCONS_%=\n\t"
    "s_mov_b64 s[98:99], exec\n\t"
    "s_lshl_b32 s26, s20, 3\n\t"
    "v_mov_b32 v4, s26\n\t"
    "s_mov_b64 exec, 1\n\t"
    "global_store_dwordx2 v4, v[2:3], s[92:93] sc1\n\t"
    "s_mov_b64 exec, 0xffff\n\t"
    "s_lshl_b32 s26, s23, 7\n\t"
    "v_lshlrev_b32 v4, 3, v1\n\t"
    "v_add_u32 v4, s26, v4\n\t"
    "LPOLL_%=:\n\t"
    "global_load_dwordx2 v[6:7], v4, s[92:93] sc1\n\t"
    "s_waitcnt vmcnt(0)\n\t"
    "v_cmp_eq_u64 vcc, v[6:7], v[2:3]\n\t"
    "s_add_u32 s96, s96, 1\n\t"
    "s_or_b32 s26, vcc_lo, s28\n\t"
    "s_cmp_gt_u32 s96, 0x40000\n\t"
    "s_cselect_b32 s27, 0xffff, 0\n\t"
    "s_or_b32 s26, s26, s27\n\t"
    "v_mov_b32 v8, s26\n\t"
    "ds_write_b32 v10, v8\n\t"
    "s_cmp_eq_u32 s26, 0xffff\n\t"
    "s_cbranch_scc0 LPOLL_%=\n\t"
    "s_mov_b64 exec, s[98:99]\n\t"
    "s_mov_b32 s26, 0xffff\n\t"
    "s_branch LISSUE_%=\n\t"
    "LCONS_%=:\n\t"
    "ds_read_b32 v8, v10\n\t"
    "s_waitcnt lgkmcnt(0)\n\t"
    "v_readfirstlane_b32 s26, v8\n\t"
    "s_or_b32 s26, s26, s28\n\t"
    "LISSUE_%=:\n\t"
    "s_andn2_b32 s26, s26, s29\n\t"
    "s_or_b32 s29, s29, s26\n\t"
    "s_bitcmp1_b32 s26, 0\n\t"
    "s_cbranch_scc0 LSK0_%=\n\t"
    "s_add_u32 s27, s78, 0\n\t"
    "buffer_load_dwordx4 v[16:19], v120, s[56:59], s27 offen sc1\n\t"
    "LSK0_%=:\n\t"
    "s_bitcmp1_b32 s26, 1\n\t"
    "s_cbranch_scc0 LSK1_%=\n\t"
    "s_add_u32 s27, s78, 4096\n\t"
    "buffer_load_dwordx4 v[20:23], v120, s[56:59], s27 offen sc1\n\t"
    "LSK1_%=:\n\t"
    "s_bitcmp1_b32 s26, 2\n\t"
    "s_cbranch_scc0 LSK2_%=\n\t"
    "s_add_u32 s27, s78, 8192\n\t"
    "buffer_load_dwordx4 v[24:27], v120, s[56:59], s27 offen sc1\n\t"
    "LSK2_%=:\n\t"
    "s_bitcmp1_b32 s26, 3\n\t"
    "s_cbranch_scc0 LSK3_%=\n\t"
    "s_add_u32 s27, s78, 12288\n\t"
    "buffer_load_dwordx4 v[28:31], v120, s[56:59], s27 offen sc1\n\t"
    "LSK3_%=:\n\t"
    "s_bitcmp1_b32 s26, 4\n\t"
    "s_cbranch_scc0 LSK4_%=\n\t"
    "s_add_u32 s27, s78, 16384\n\t"
    "buffer_load_dwordx4 v[32:35], v120, s[56:59], s27 offen sc1\n\t"
    "LSK4_%=:\n\t"
    "s_bitcmp1_b32 s26, 5\n\t"
    "s_cbranch_scc0 LSK5_%=\n\t"
    "s_add_u32 s27, s78, 20480\n\t"
    "buffer_load_dwordx4 v[36:39], v120, s[56:59], s27 offen sc1\n\t"
    "LSK5_%=:\n\t"
    "s_bitcmp1_b32 s26, 6\n\t"
    "s_cbranch_scc0 LSK6_%=\n\t"
    "s_add_u32 s27, s78, 24576\n\t"
    "buffer_load_dwordx4 v[40:43], v120, s[56:59], s27 offen sc1\n\t"
    "LSK6_%=:\n\t"
    "s_bitcmp1_b32 s26, 7\n\t"
    "s_cbranch_scc0 LSK7_%=\n\t"
    "s_add_u32 s27, s78, 28672\n\t"
    "buffer_load_dwordx4 v[44:47], v120, s[56:59], s27 offen sc1\n\t"
    "LSK7_%=:\n\t"
    "s_bitcmp1_b32 s26, 8\n\t"
    "s_cbranch_scc0 LSK8_%=\n\t"
    "s_add_u32 s27, s78, 32768\n\t"
    "buffer_load_dwordx4 v[48:51], v120, s[56:59], s27 offen sc1\n\t"
    "LSK8_%=:\n\t"
    "s_bitcmp1_b32 s26, 9\n\t"
    "s_cbranch_scc0 LSK9_%=\n\t"
    "s_add_u32 s27, s78, 36864\n\t"
    "buffer_load_dwordx4 v[52:55], v120, s[56:59], s27 offen sc1\n\t"
    "LSK9_%=:\n\t"
    "s_bitcmp1_b32 s26, 10\n\t"
    "s_cbranch_scc0 LSK10_%=\n\t"
    "s_add_u32 s27, s78, 40960\n\t"
    "buffer_load_dwordx4 v[56:59], v120, s[56:59], s27 offen sc1\n\t"
    "LSK10_%=:\n\t"
    "s_bitcmp1_b32 s26, 11\n\t"
    "s_cbranch_scc0 LSK11_%=\n\t"
    "s_add_u32 s27, s78, 45056\n\t"
    "buffer_load_dwordx4 v[60:63], v120, s[56:59], s27 offen sc1\n\t"
    "LSK11_%=:\n\t"
    "s_bitcmp1_b32 s26, 12\n\t"
    "s_cbranch_scc0 LSK12_%=\n\t"
    "s_add_u32 s27, s78, 49152\n\t"
    "buffer_load_dwordx4 v[64:67], v120, s[56:59], s27 offen sc1\n\t"
    "LSK12_%=:\n\t"
    "s_bitcmp1_b32 s26, 13\n\t"
    "s_cbranch_scc0 LSK13_%=\n\t"
    "s_add_u32 s27, s78, 53248\n\t"
    "buffer_load_dwordx4 v[68:71], v120, s[56:59], s27 offen sc1\n\t"
    "LSK13_%=:\n\t"
    "s_bitcmp1_b32 s26, 14\n\t"
    "s_cbranch_scc0 LSK14_%=\n\t"
    "s_add_u32 s27, s78, 57344\n\t"
    "buffer_load_dwordx4 v[72:75], v120, s[56:59], s27 offen sc1\n\t"
    "LSK14_%=:\n\t"
    "s_bitcmp1_b32 s26, 15\n\t"
    "s_cbranch_scc0 LSK15_%=\n\t"
    "s_add_u32 s27, s78, 61440\n\t"
    "buffer_load_dwordx4 v[76:79], v120, s[56:59], s27 offen sc1\n\t"
    "LSK15_%=:\n\t"
    "s_cmp_eq_u32 s29, 0xffff\n\t"
    "s_cbranch_scc1 LPOLLD_%=\n\t"
    "s_add_u32 s96, s96, 1\n\t"
    "s_cmp_gt_u32 s96, 0x1000000\n\t"
    "s_cselect_b32 s28, 0xffff, s28\n\t"
    "s_sleep 1\n\t"
    "s_branch LCONS_%=\n\t"
    "LPOLLD_%=:\n\t"
    "v_and_b32 v2, 31, v1\n\t"
    "v_lshrrev_b32 v3, 5, v1\n\t"
    "v_lshlrev_b32 v2, 10, v2\n\t"
    "v_lshl_add_u32 v2, v3, 4, v2\n\t"
    "s_and_b32 s26, s20, 15\n\t"
    "s_lshl_b32 s26, s26, 6\n\t"
    "s_lshl_b32 s27, s83, 5\n\t"
    "s_add_u32 s26, s26, s27\n\t"
    "s_lshl_b32 s27, s82, 15\n\t"
    "s_add_u32 s26, s26, s27\n\t"
    "s_mov_b32 s79, 0xffff0000\n\t"
    "s_waitcnt vmcnt(0)\n\t"
    "v_lshlrev_b32 v96, 16, v16\n\t"
    "v_and_b32 v97, s79, v16\n\t"
    "v_lshlrev_b32 v98, 16, v17\n\t"
    "v_and_b32 v99, s79, v17\n\t"
    "v_lshlrev_b32 v100, 16, v18\n\t"
    "v_and_b32 v101, s79, v18\n\t"
    "v_lshlrev_b32 v102, 16, v19\n\t"
    "v_and_b32 v103, s79, v19\n\t"
    "v_lshlrev_b32 v108, 16, v20\n\t"
    "v_and_b32 v109, s79, v20\n\t"
    "v_pk_add_f32 v[96:97], v[96:97], v[108:109]\n\t"
    "v_lshlrev_b32 v110, 16, v21\n\t"
    "v_and_b32 v111, s79, v21\n\t"
    "v_pk_add_f32 v[98:99], v[98:99], v[110:111]\n\t"
    "v_lshlrev_b32 v108, 16, v22\n\t"
    "v_and_b32 v109, s79, v22\n\t"
    "v_pk_add_f32 v[100:101], v[100:101], v[108:109]\n\t"
    "v_lshlrev_b32 v110, 16, v23\n\t"
    "v_and_b32 v111, s79, v23\n\t"
    "v_pk_add_f32 v[102:103], v[102:103], v[110:111]\n\t"
    "v_lshlrev_b32 v108, 16, v24\n\t"
    "v_and_b32 v109, s79, v24\n\t"
    "v_pk_add_f32 v[96:97], v[96:97], v[108:109]\n\t"
    "v_lshlrev_b32 v110, 16, v25\n\t"
    "v_and_b32 v111, s79, v25\n\t"
    "v_pk_add_f32 v[98:99], v[98:99], v[110:111]\n\t"
    "v_lshlrev_b32 v108, 16, v26\n\t"
    "v_and_b32 v109, s79, v26\n\t"
    "v_pk_add_f32 v[100:101], v[100:101], v[108:109]\n\t"
    "v_lshlrev_b32 v110, 16, v27\n\t"
    "v_and_b32 v111, s79, v27\n\t"
    "v_pk_add_f32 v[102:103], v[102:103], v[110:111]\n\t"
    "v_lshlrev_b32 v108, 16, v28\n\t"
    "v_and_b32 v109, s79, v28\n\t"
    "v_pk_add_f32 v[96:97], v[96:97], v[108:109]\n\t"
    "v_lshlrev_b32 v110, 16, v29\n\t"
    "v_and_b32 v111, s79, v29\n\t"
    "v_pk_add_f32 v[98:99], v[98:99], v[110:111]\n\t"
    "v_lshlrev_b32 v108, 16, v30\n\t"
    "v_and_b32 v109, s79, v30\n\t"
    "v_pk_add_f32 v[100:101], v[100:101], v[108:109]\n\t"
    "v_lshlrev_b32 v110, 16, v31\n\t"
    "v_and_b32 v111, s79, v31\n\t"
    "v_pk_add_f32 v[102:103], v[102:103], v[110:111]\n\t"
    "v_lshlrev_b32 v108, 16, v32\n\t"
    "v_and_b32 v109, s79, v32\n\t"
    "v_pk_add_f32 v[96:97], v[96:97], v[108:109]\n\t"
    "v_lshlrev_b32 v110, 16, v33\n\t"
    "v_and_b32 v111, s79, v33\n\t"
    "v_pk_add_f32 v[98:99], v[98:99], v[110:111]\n\t"
    "v_lshlrev_b32 v108, 16, v34\n\t"
    "v_and_b32 v109, s79, v34\n\t"
    "v_pk_add_f32 v[100:101], v[100:101], v[108:109]\n\t"
    "v_lshlrev_b32 v110, 16, v35\n\t"
    "v_and_b32 v111, s79, v35\n\t"
    "v_pk_add_f32 v[102:103], v[102:103], v[110:111]\n\t"
    "v_lshlrev_b32 v108, 16, v36\n\t"
    "v_and_b32 v109, s79, v36\n\t"
    "v_pk_add_f32 v[96:97], v[96:97], v[108:109]\n\t"
    "v_lshlrev_b32 v110, 16, v37\n\t"
    "v_and_b32 v111, s79, v37\n\t"
    "v_pk_add_f32 v[98:99], v[98:99], v[110:111]\n\t"
    "v_lshlrev_b32 v108, 16, v38\n\t"
    "v_and_b32 v109, s79, v38\n\t"
    "v_pk_add_f32 v[100:101], v[100:101], v[108:109]\n\t"
    "v_lshlrev_b32 v110, 16, v39\n\t"
    "v_and_b32 v111, s79, v39\n\t"
    "v_pk_add_f32 v[102:103], v[102:103], v[110:111]\n\t"
    "v_lshlrev_b32 v108, 16, v40\n\t"
    "v_and_b32 v109, s79, v40\n\t"
    "v_pk_add_f32 v[96:97], v[96:97], v[108:109]\n\t"
    "v_lshlrev_b32 v110, 16, v41\n\t"
    "v_and_b32 v111, s79, v41\n\t"
    "v_pk_add_f32 v[98:99], v[98:99], v[110:111]\n\t"
    "v_lshlrev_b32 v108, 16, v42\n\t"
    "v_and_b32 v109, s79, v42\n\t"
    "v_pk_add_f32 v[100:101], v[100:101], v[108:109]\n\t"
    "v_lshlrev_b32 v110, 16, v43\n\t"
    "v_and_b32 v111, s79, v43\n\t"
    "v_pk_add_f32 v[102:103], v[102:103], v[110:111]\n\t"
    "v_lshlrev_b32 v108, 16, v44\n\t"
    "v_and_b32 v109, s79, v44\n\t"
    "v_pk_add_f32 v[96:97], v[96:97], v[108:109]\n\t"
    "v_lshlrev_b32 v110, 16, v45\n\t"
    "v_and_b32 v111, s79, v45\n\t"
    "v_pk_add_f32 v[98:99], v[98:99], v[110:111]\n\t"
    "v_lshlrev_b32 v108, 16, v46\n\t"
    "v_and_b32 v109, s79, v46\n\t"
    "v_pk_add_f32 v[100:101], v[100:101], v[108:109]\n\t"
    "v_lshlrev_b32 v110, 16, v47\n\t"
    "v_and_b32 v111, s79, v47\n\t"
    "v_pk_add_f32 v[102:103], v[102:103], v[110:111]\n\t"
    "v_lshlrev_b32 v108, 16, v48\n\t"
    "v_and_b32 v109, s79, v48\n\t"
    "v_pk_add_f32 v[96:97], v[96:97], v[108:109]\n\t"
    "v_lshlrev_b32 v110, 16, v49\n\t"
    "v_and_b32 v111, s79, v49\n\t"
    "v_pk_add_f32 v[98:99], v[98:99], v[110:111]\n\t"
    "v_lshlrev_b32 v108, 16, v50\n\t"
    "v_and_b32 v109, s79, v50\n\t"
    "v_pk_add_f32 v[100:101], v[100:101], v[108:109]\n\t"
    "v_lshlrev_b32 v110, 16, v51\n\t"
    "v_and_b32 v111, s79, v51\n\t"
    "v_pk_add_f32 v[102:103], v[102:103], v[110:111]\n\t"
    "v_lshlrev_b32 v108, 16, v52\n\t"
    "v_and_b32 v109, s79, v52\n\t"
    "v_pk_add_f32 v[96:97], v[96:97], v[108:109]\n\t"
    "v_lshlrev_b32 v110, 16, v53\n\t"
    "v_and_b32 v111, s79, v53\n\t"
    "v_pk_add_f32 v[98:99], v[98:99], v[110:111]\n\t"
    "v_lshlrev_b32 v108, 16, v54\n\t"
    "v_and_b32 v109, s79, v54\n\t"
    "v_pk_add_f32 v[100:101], v[100:101], v[108:109]\n\t"
    "v_lshlrev_b32 v110, 16, v55\n\t"
    "v_and_b32 v111, s79, v55\n\t"
    "v_pk_add_f32 v[102:103], v[102:103], v[110:111]\n\t"
    "v_lshlrev_b32 v108, 16, v56\n\t"
    "v_and_b32 v109, s79, v56\n\t"
    "v_pk_add_f32 v[96:97], v[96:97], v[108:109]\n\t"
    "v_lshlrev_b32 v110, 16, v57\n\t"
    "v_and_b32 v111, s79, v57\n\t"
    "v_pk_add_f32 v[98:99], v[98:99], v[110:111]\n\t"
    "v_lshlrev_b32 v108, 16, v58\n\t"
    "v_and_b32 v109, s79, v58\n\t"
    "v_pk_add_f32 v[100:101], v[100:101], v[108:109]\n\t"
    "v_lshlrev_b32 v110, 16, v59\n\t"
    "v_and_b32 v111, s79, v59\n\t"
    "v_pk_add_f32 v[102:103], v[102:103], v[110:111]\n\t"
    "v_lshlrev_b32 v108, 16, v60\n\t"
    "v_and_b32 v109, s79, v60\n\t"
    "v_pk_add_f32 v[96:97], v[96:97], v[108:109]\n\t"
    "v_lshlrev_b32 v110, 16, v61\n\t"
    "v_and_b32 v111, s79, v61\n\t"
    "v_pk_add_f32 v[98:99], v[98:99], v[110:111]\n\t"
    "v_lshlrev_b32 v108, 16, v62\n\t"
    "v_and_b32 v109, s79, v62\n\t"
    "v_pk_add_f32 v[100:101], v[100:101], v[108:109]\n\t"
    "v_lshlrev_b32 v110, 16, v63\n\t"
    "v_and_b32 v111, s79, v63\n\t"
    "v_pk_add_f32 v[102:103], v[102:103], v[110:111]\n\t"
    "v_lshlrev_b32 v108, 16, v64\n\t"
    "v_and_b32 v109, s79, v64\n\t"
    "v_pk_add_f32 v[96:97], v[96:97], v[108:109]\n\t"
    "v_lshlrev_b32 v110, 16, v65\n\t"
    "v_and_b32 v111, s79, v65\n\t"
    "v_pk_add_f32 v[98:99], v[98:99], v[110:111]\n\t"
    "v_lshlrev_b32 v108, 16, v66\n\t"
    "v_and_b32 v109, s79, v66\n\t"
    "v_pk_add_f32 v[100:101], v[100:101], v[108:109]\n\t"
    "v_lshlrev_b32 v110, 16, v67\n\t"
    "v_and_b32 v111, s79, v67\n\t"
    "v_pk_add_f32 v[102:103], v[102:103], v[110:111]\n\t"
    "v_lshlrev_b32 v108, 16, v68\n\t"
    "v_and_b32 v109, s79, v68\n\t"
    "v_pk_add_f32 v[96:97], v[96:97], v[108:109]\n\t"
    "v_lshlrev_b32 v110, 16, v69\n\t"
    "v_and_b32 v111, s79, v69\n\t"
    "v_pk_add_f32 v[98:99], v[98:99], v[110:111]\n\t"
    "v_lshlrev_b32 v108, 16, v70\n\t"
    "v_and_b32 v109, s79, v70\n\t"
    "v_pk_add_f32 v[100:101], v[100:101], v[108:109]\n\t"
    "v_lshlrev_b32 v110, 16, v71\n\t"
    "v_and_b32 v111, s79, v71\n\t"
    "v_pk_add_f32 v[102:103], v[102:103], v[110:111]\n\t"
    "v_lshlrev_b32 v108, 16, v72\n\t"
    "v_and_b32 v109, s79, v72\n\t"
    "v_pk_add_f32 v[96:97], v[96:97], v[108:109]\n\t"
    "v_lshlrev_b32 v110, 16, v73\n\t"
    "v_and_b32 v111, s79, v73\n\t"
    "v_pk_add_f32 v[98:99], v[98:99], v[110:111]\n\t"
    "v_lshlrev_b32 v108, 16, v74\n\t"
    "v_and_b32 v109, s79, v74\n\t"
    "v_pk_add_f32 v[100:101], v[100:101], v[108:109]\n\t"
    "v_lshlrev_b32 v110, 16, v75\n\t"
    "v_and_b32 v111, s79, v75\n\t"
    "v_pk_add_f32 v[102:103], v[102:103], v[110:111]\n\t"
    "v_lshlrev_b32 v108, 16, v76\n\t"
    "v_and_b32 v109, s79, v76\n\t"
    "v_pk_add_f32 v[96:97], v[96:97], v[108:109]\n\t"
    "v_lshlrev_b32 v110, 16, v77\n\t"
    "v_and_b32 v111, s79, v77\n\t"
    "v_pk_add_f32 v[98:99], v[98:99], v[110:111]\n\t"
    "v_lshlrev_b32 v108, 16, v78\n\t"
    "v_and_b32 v109, s79, v78\n\t"
    "v_pk_add_f32 v[100:101], v[100:101], v[108:109]\n\t"
    "v_lshlrev_b32 v110, 16, v79\n\t"
    "v_and_b32 v111, s79, v79\n\t"
    "v_pk_add_f32 v[102:103], v[102:103], v[110:111]\n\t"
    "v_cvt_pk_bf16_f32 v104, v96, v97\n\t"
    "v_cvt_pk_bf16_f32 v105, v98, v99\n\t"
    "v_cvt_pk_bf16_f32 v106, v100, v101\n\t"
    "v_cvt_pk_bf16_f32 v107, v102, v103\n\t"
    "buffer_store_dwordx4 v[104:107], v2, s[68:71], s26 offen\n\t"
    "s_or_b32 s26, s20, s21\n\t"
    "s_cmp_lg_u32 s26, 0\n\t"
    "s_cbranch_scc1 LEND_%=\n\t"
    "v_mov_b32 v2, s94\n\t"
    "v_mov_b32 v3, s95\n\t"
    "v_lshlrev_b32 v4, 3, v1\n\t"
    "s_mov_b32 s96, 0\n\t"
    "LPOLL2_%=:\n\t"
    "global_load_dwordx2 v[6:7], v4, s[92:93] sc1\n\t"
    "v_add_u32 v5, 512, v4\n\t"
    "global_load_dwordx2 v[8:9], v5, s[92:93] sc1\n\t"
    "v_add_u32 v5, 1024, v4\n\t"
    "global_load_dwordx2 v[10:11], v5, s[92:93] sc1\n\t"
    "v_add_u32 v5, 1536, v4\n\t"
    "global_load_dwordx2 v[12:13], v5, s[92:93] sc1\n\t"
    "s_waitcnt vmcnt(0)\n\t"
    "v_cmp_eq_u64 vcc, v[6:7], v[2:3]\n\t"
    "v_cmp_eq_u64 s[98:99], v[8:9], v[2:3]\n\t"
    "s_nop 0\n\t"
    "s_and_b64 vcc, vcc, s[98:99]\n\t"
    "v_cmp_eq_u64 s[98:99], v[10:11], v[2:3]\n\t"
    "s_nop 0\n\t"
    "s_and_b64 vcc, vcc, s[98:99]\n\t"
    "v_cmp_eq_u64 s[98:99], v[12:13], v[2:3]\n\t"
    "s_nop 0\n\t"
    "s_and_b64 vcc, vcc, s[98:99]\n\t"
    "s_add_u32 s96, s96, 1\n\t"
    "s_cmp_eq_u64 vcc, exec\n\t"
    "s_cbranch_scc1 LPOLL2D_%=\n\t"
    "s_cmp_gt_u32 s96, 0x40000\n\t"
    "s_cbranch_scc1 LPOLL2D_%=\n\t"
    "s_sleep 1\n\t"
    "s_branch LPOLL2_%=\n\t"
    "LPOLL2D_%=:\n\t"
    "v_mov_b32 v4, 65536\n\t"
    "s_mov_b64 exec, 1\n\t"
    "global_store_dwordx2 v4, v[2:3], s[92:93] sc1\n\t"
    "s_branch LEND_%=\n\t"
    "LSLOW_%=:\n\t"
    "s_waitcnt vmcnt(0) lgkmcnt(0)\n\t"
    "s_barrier\n\t"
    "s_mov_b32 s97, 1\n\t"
    "s_mov_b32 s29, 0\n\t"
    "v_mov_b32 v113, 0xff61b1e6\n\t"
    "v_mov_b32 v80, 0\n\t"
    "v_mov_b32 v160, 0\n\t"
    "v_mov_b32 v161, 0\n\t"
    "v_mov_b32 v162, 0\n\t"
    "v_mov_b32 v163, 0\n\t"
    "v_mov_b32 v164, 0\n\t"
    "v_mov_b32 v165, 0\n\t"
    "v_mov_b32 v166, 0\n\t"
    "v_mov_b32 v167, 0\n\t"
    "v_mov_b32 v168, 0\n\t"
    "v_mov_b32 v169, 0\n\t"
    "v_mov_b32 v170, 0\n\t"
    "v_mov_b32 v171, 0\n\t"
    "v_mov_b32 v172, 0\n\t"
    "v_mov_b32 v173, 0\n\t"
    "v_mov_b32 v174, 0\n\t"
    "v_mov_b32 v175, 0\n\t"
    "v_mov_b32 v176, 0\n\t"
    "v_mov_b32 v177, 0\n\t"
    "v_mov_b32 v178, 0\n\t"
    "v_mov_b32 v179, 0\n\t"
    "v_mov_b32 v180, 0\n\t"
    "v_mov_b32 v181, 0\n\t"
    "v_mov_b32 v182, 0\n\t"
    "v_mov_b32 v183, 0\n\t"
    "v_mov_b32 v184, 0\n\t"
    "v_mov_b32 v185, 0\n\t"
    "v_mov_b32 v186, 0\n\t"
    "v_mov_b32 v187, 0\n\t"
    "v_mov_b32 v188, 0\n\t"
    "v_mov_b32 v189, 0\n\t"
    "v_mov_b32 v190, 0\n\t"
    "v_mov_b32 v191, 0\n\t"
    "v_mov_b32 v192, 0\n\t"
    "v_mov_b32 v193, 0\n\t"
    "v_mov_b32 v194, 0\n\t"
    "v_mov_b32 v195, 0\n\t"
    "v_mov_b32 v196, 0\n\t"
    "v_mov_b32 v197, 0\n\t"
    "v_mov_b32 v198, 0\n\t"
    "v_mov_b32 v199, 0\n\t"
    "v_mov_b32 v200, 0\n\t"
    "v_mov_b32 v201, 0\n\t"
    "v_mov_b32 v202, 0\n\t"
    "v_mov_b32 v203, 0\n\t"
    "v_mov_b32 v204, 0\n\t"
    "v_mov_b32 v205, 0\n\t"
    "v_mov_b32 v206, 0\n\t"
    "v_mov_b32 v207, 0\n\t"
    "v_mov_b32 v208, 0\n\t"
    "v_mov_b32 v209, 0\n\t"
    "v_mov_b32 v210, 0\n\t"
    "v_mov_b32 v211, 0\n\t"
    "v_mov_b32 v212, 0\n\t"
    "v_mov_b32 v213, 0\n\t"
    "v_mov_b32 v214, 0\n\t"
    "v_mov_b32 v215, 0\n\t"
    "v_mov_b32 v216, 0\n\t"
    "v_mov_b32 v217, 0\n\t"
    "v_mov_b32 v218, 0\n\t"
    "v_mov_b32 v219, 0\n\t"
    "v_mov_b32 v220, 0\n\t"
    "v_mov_b32 v221, 0\n\t"
    "v_mov_b32 v222, 0\n\t"
    "v_mov_b32 v223, 0\n\t"
    "LSLOOP_%=:\n\t"
    "s_lshl_b32 s26, s29, 14\n\t"
    "s_add_u32 s76, s75, s26\n\t"
    "s_add_u32 m0, s75, 65536\n\t"
    "s_nop 0\n\t"
    "buffer_load_dwordx4 v106, s[44:47], s76 offen lds\n\t"
    "buffer_load_dwordx4 v107, s[44:47], s76 offen offset:1024 lds\n\t"
    "buffer_load_dwordx4 v108, s[44:47], s76 offen offset:2048 lds\n\t"
    "buffer_load_dwordx4 v109, s[44:47], s76 offen offset:3072 lds\n\t"
    "s_add_u32 m0, s75, 81920\n\t"
    "s_nop 0\n\t"
    "buffer_load_dwordx4 v110, s[48:51], s76 offen lds\n\t"
    "buffer_load_dwordx4 v110, s[48:51], s76 offen offset:1024 lds\n\t"
    "buffer_load_dwordx4 v110, s[48:51], s76 offen offset:2048 lds\n\t"
    "buffer_load_dwordx4 v110, s[48:51], s76 offen offset:3072 lds\n\t"
    "s_waitcnt vmcnt(0)\n\t"
    "s_barrier\n\t"
    "ds_read_b128 a[0:3], v90\n\t"
    "ds_read_b128 a[4:7], v91\n\t"
    "ds_read_b128 a[8:11], v92\n\t"
    "ds_read_b128 a[12:15], v93\n\t"
    "ds_read_b128 a[16:19], v94\n\t"
    "ds_read_b128 a[20:23], v95\n\t"
    "ds_read_b128 a[24:27], v96\n\t"
    "ds_read_b128 a[28:31], v97\n\t"
    "ds_read_b128 a[32:35], v90 offset:8192\n\t"
    "ds_read_b128 a[36:39], v91 offset:8192\n\t"
    "ds_read_b128 a[40:43], v92 offset:8192\n\t"
    "ds_read_b128 a[44:47], v93 offset:8192\n\t"
    "ds_read_b128 a[48:51], v94 offset:8192\n\t"
    "ds_read_b128 a[52:55], v95 offset:8192\n\t"
    "ds_read_b128 a[56:59], v96 offset:8192\n\t"
    "ds_read_b128 a[60:63], v97 offset:8192\n\t"
    "s_waitcnt lgkmcnt(0)\n\t"
    "ds_read_b64_tr_b16 a[64:65], v102 offset:16384\n\t"
    "ds_read_b64_tr_b16 a[66:67], v102 offset:18432\n\t"
    "ds_read_b64_tr_b16 a[68:69], v103 offset:16384\n\t"
    "ds_read_b64_tr_b16 a[70:71], v103 offset:18432\n\t"
    "ds_read_b64_tr_b16 a[72:73], v104 offset:16384\n\t"
    "ds_read_b64_tr_b16 a[74:75], v104 offset:18432\n\t"
    "ds_read_b64_tr_b16 a[76:77], v105 offset:16384\n\t"
    "ds_read_b64_tr_b16 a[78:79], v105 offset:18432\n\t"
    "ds_read_b64_tr_b16 a[80:81], v102 offset:20480\n\t"
    "ds_read_b64_tr_b16 a[82:83], v102 offset:22528\n\t"
    "ds_read_b64_tr_b16 a[84:85], v103 offset:20480\n\t"
    "ds_read_b64_tr_b16 a[86:87], v103 offset:22528\n\t"
    "ds_read_b64_tr_b16 a[88:89], v104 offset:20480\n\t"
    "ds_read_b64_tr_b16 a[90:91], v104 offset:22528\n\t"
    "ds_read_b64_tr_b16 a[92:93], v105 offset:20480\n\t"
    "ds_read_b64_tr_b16 a[94:95], v105 offset:22528\n\t"
    "s_waitcnt lgkmcnt(0)\n\t"
    "ds_read_b64_tr_b16 a[96:97], v102 offset:24576\n\t"
    "ds_read_b64_tr_b16 a[98:99], v102 offset:26624\n\t"
    "ds_read_b64_tr_b16 a[100:101], v103 offset:24576\n\t"
    "ds_read_b64_tr_b16 a[102:103], v103 offset:26624\n\t"
    "ds_read_b64_tr_b16 a[104:105], v104 offset:24576\n\t"
    "ds_read_b64_tr_b16 a[106:107], v104 offset:26624\n\t"
    "ds_read_b64_tr_b16 a[108:109], v105 offset:24576\n\t"
    "ds_read_b64_tr_b16 a[110:111], v105 offset:26624\n\t"
    "ds_read_b64_tr_b16 a[112:113], v102 offset:28672\n\t"
    "ds_read_b64_tr_b16 a[114:115], v102 offset:30720\n\t"
    "ds_read_b64_tr_b16 a[116:117], v103 offset:28672\n\t"
    "ds_read_b64_tr_b16 a[118:119], v103 offset:30720\n\t"
    "ds_read_b64_tr_b16 a[120:121], v104 offset:28672\n\t"
    "ds_read_b64_tr_b16 a[122:123], v104 offset:30720\n\t"
    "ds_read_b64_tr_b16 a[124:125], v105 offset:28672\n\t"
    "ds_read_b64_tr_b16 a[126:127], v105 offset:30720\n\t"
    "v_mfma_f32_32x32x16_bf16 v[16:31], a[0:3], v[128:131], 0\n\t"
    "v_mfma_f32_32x32x16_bf16 v[16:31], a[4:7], v[132:135], v[16:31]\n\t"
    "v_mfma_f32_32x32x16_bf16 v[16:31], a[8:11], v[136:139], v[16:31]\n\t"
    "v_mfma_f32_32x32x16_bf16 v[16:31], a[12:15], v[140:143], v[16:31]\n\t"
    "v_mfma_f32_32x32x16_bf16 v[16:31], a[16:19], v[144:147], v[16:31]\n\t"
    "v_mfma_f32_32x32x16_bf16 v[16:31], a[20:23], v[148:151], v[16:31]\n\t"
    "v_mfma_f32_32x32x16_bf16 v[16:31], a[24:27], v[152:155], v[16:31]\n\t"
    "v_mfma_f32_32x32x16_bf16 v[16:31], a[28:31], v[156:159], v[16:31]\n\t"
    "v_mfma_f32_32x32x16_bf16 v[32:47], a[32:35], v[128:131], 0\n\t"
    "v_mfma_f32_32x32x16_bf16 v[32:47], a[36:39], v[132:135], v[32:47]\n\t"
    "v_mfma_f32_32x32x16_bf16 v[32:47], a[40:43], v[136:139], v[32:47]\n\t"
    "v_mfma_f32_32x32x16_bf16 v[32:47], a[44:47], v[140:143], v[32:47]\n\t"
    "v_mfma_f32_32x32x16_bf16 v[32:47], a[48:51], v[144:147], v[32:47]\n\t"
    "v_mfma_f32_32x32x16_bf16 v[32:47], a[52:55], v[148:151], v[32:47]\n\t"
    "v_mfma_f32_32x32x16_bf16 v[32:47], a[56:59], v[152:155], v[32:47]\n\t"
    "v_mfma_f32_32x32x16_bf16 v[32:47], a[60:63], v[156:159], v[32:47]\n\t"
    "s_nop 15\n\t"
    "v_max_f32 v2, v16, v32\n\t"
    "v_max3_f32 v2, v2, v17, v33\n\t"
    "v_max3_f32 v2, v2, v18, v34\n\t"
    "v_max3_f32 v2, v2, v19, v35\n\t"
    "v_max3_f32 v2, v2, v20, v36\n\t"
    "v_max3_f32 v2, v2, v21, v37\n\t"
    "v_max3_f32 v2, v2, v22, v38\n\t"
    "v_max3_f32 v2, v2, v23, v39\n\t"
    "v_max3_f32 v2, v2, v24, v40\n\t"
    "v_max3_f32 v2, v2, v25, v41\n\t"
    "v_max3_f32 v2, v2, v26, v42\n\t"
    "v_max3_f32 v2, v2, v27, v43\n\t"
    "v_max3_f32 v2, v2, v28, v44\n\t"
    "v_max3_f32 v2, v2, v29, v45\n\t"
    "v_max3_f32 v2, v2, v30, v46\n\t"
    "v_max3_f32 v2, v2, v31, v47\n\t"
    "v_mov_b32 v3, v2\n\t"
    "s_nop 1\n\t"
    "v_permlane32_swap_b32 v2, v3\n\t"
    "v_max_f32 v2, v2, v3\n\t"
    "v_max_f32 v2, v2, v113\n\t"
    "v_sub_f32 v3, v113, v2\n\t"
    "v_exp_f32 v3, v3\n\t"
    "v_mov_b32 v113, v2\n\t"
    "s_nop 0\n\t"
    "v_mul_f32 v80, v80, v3\n\t"
    "v_mul_f32 v160, v160, v3\n\t"
    "v_mul_f32 v161, v161, v3\n\t"
    "v_mul_f32 v162, v162, v3\n\t"
    "v_mul_f32 v163, v163, v3\n\t"
    "v_mul_f32 v164, v164, v3\n\t"
    "v_mul_f32 v165, v165, v3\n\t"
    "v_mul_f32 v166, v166, v3\n\t"
    "v_mul_f32 v167, v167, v3\n\t"
    "v_mul_f32 v168, v168, v3\n\t"
    "v_mul_f32 v169, v169, v3\n\t"
    "v_mul_f32 v170, v170, v3\n\t"
    "v_mul_f32 v171, v171, v3\n\t"
    "v_mul_f32 v172, v172, v3\n\t"
    "v_mul_f32 v173, v173, v3\n\t"
    "v_mul_f32 v174, v174, v3\n\t"
    "v_mul_f32 v175, v175, v3\n\t"
    "v_mul_f32 v176, v176, v3\n\t"
    "v_mul_f32 v177, v177, v3\n\t"
    "v_mul_f32 v178, v178, v3\n\t"
    "v_mul_f32 v179, v179, v3\n\t"
    "v_mul_f32 v180, v180, v3\n\t"
    "v_mul_f32 v181, v181, v3\n\t"
    "v_mul_f32 v182, v182, v3\n\t"
    "v_mul_f32 v183, v183, v3\n\t"
    "v_mul_f32 v184, v184, v3\n\t"
    "v_mul_f32 v185, v185, v3\n\t"
    "v_mul_f32 v186, v186, v3\n\t"
    "v_mul_f32 v187, v187, v3\n\t"
    "v_mul_f32 v188, v188, v3\n\t"
    "v_mul_f32 v189, v189, v3\n\t"
    "v_mul_f32 v190, v190, v3\n\t"
    "v_mul_f32 v191, v191, v3\n\t"
    "v_mul_f32 v192, v192, v3\n\t"
    "v_mul_f32 v193, v193, v3\n\t"
    "v_mul_f32 v194, v194, v3\n\t"
    "v_mul_f32 v195, v195, v3\n\t"
    "v_mul_f32 v196, v196, v3\n\t"
    "v_mul_f32 v197, v197, v3\n\t"
    "v_mul_f32 v198, v198, v3\n\t"
    "v_mul_f32 v199, v199, v3\n\t"
    "v_mul_f32 v200, v200, v3\n\t"
    "v_mul_f32 v201, v201, v3\n\t"
    "v_mul_f32 v202, v202, v3\n\t"
    "v_mul_f32 v203, v203, v3\n\t"
    "v_mul_f32 v204, v204, v3\n\t"
    "v_mul_f32 v205, v205, v3\n\t"
    "v_mul_f32 v206, v206, v3\n\t"
    "v_mul_f32 v207, v207, v3\n\t"
    "v_mul_f32 v208, v208, v3\n\t"
    "v_mul_f32 v209, v209, v3\n\t"
    "v_mul_f32 v210, v210, v3\n\t"
    "v_mul_f32 v211, v211, v3\n\t"
    "v_mul_f32 v212, v212, v3\n\t"
    "v_mul_f32 v213, v213, v3\n\t"
    "v_mul_f32 v214, v214, v3\n\t"
    "v_mul_f32 v215, v215, v3\n\t"
    "v_mul_f32 v216, v216, v3\n\t"
    "v_mul_f32 v217, v217, v3\n\t"
    "v_mul_f32 v218, v218, v3\n\t"
    "v_mul_f32 v219, v219, v3\n\t"
    "v_mul_f32 v220, v220, v3\n\t"
    "v_mul_f32 v221, v221, v3\n\t"
    "v_mul_f32 v222, v222, v3\n\t"
    "v_mul_f32 v223, v223, v3\n\t"
    "v_sub_f32 v16, v16, v2\n\t"
    "v_sub_f32 v17, v17, v2\n\t"
    "v_sub_f32 v18, v18, v2\n\t"
    "v_sub_f32 v19, v19, v2\n\t"
    "v_sub_f32 v20, v20, v2\n\t"
    "v_sub_f32 v21, v21, v2\n\t"
    "v_sub_f32 v22, v22, v2\n\t"
    "v_sub_f32 v23, v23, v2\n\t"
    "v_sub_f32 v24, v24, v2\n\t"
    "v_sub_f32 v25, v25, v2\n\t"
    "v_sub_f32 v26, v26, v2\n\t"
    "v_sub_f32 v27, v27, v2\n\t"
    "v_sub_f32 v28, v28, v2\n\t"
    "v_sub_f32 v29, v29, v2\n\t"
    "v_sub_f32 v30, v30, v2\n\t"
    "v_sub_f32 v31, v31, v2\n\t"
    "v_exp_f32 v16, v16\n\t"
    "v_exp_f32 v17, v17\n\t"
    "v_exp_f32 v18, v18\n\t"
    "v_exp_f32 v19, v19\n\t"
    "v_exp_f32 v20, v20\n\t"
    "v_exp_f32 v21, v21\n\t"
    "v_exp_f32 v22, v22\n\t"
    "v_exp_f32 v23, v23\n\t"
    "v_exp_f32 v24, v24\n\t"
    "v_exp_f32 v25, v25\n\t"
    "v_exp_f32 v26, v26\n\t"
    "v_exp_f32 v27, v27\n\t"
    "v_exp_f32 v28, v28\n\t"
    "v_exp_f32 v29, v29\n\t"
    "v_exp_f32 v30, v30\n\t"
    "v_exp_f32 v31, v31\n\t"
    "s_nop 0\n\t"
    "v_add_f32 v80, v80, v16\n\t"
    "v_add_f32 v80, v80, v17\n\t"
    "v_add_f32 v80, v80, v18\n\t"
    "v_add_f32 v80, v80, v19\n\t"
    "v_add_f32 v80, v80, v20\n\t"
    "v_add_f32 v80, v80, v21\n\t"
    "v_add_f32 v80, v80, v22\n\t"
    "v_add_f32 v80, v80, v23\n\t"
    "v_add_f32 v80, v80, v24\n\t"
    "v_add_f32 v80, v80, v25\n\t"
    "v_add_f32 v80, v80, v26\n\t"
    "v_add_f32 v80, v80, v27\n\t"
    "v_add_f32 v80, v80, v28\n\t"
    "v_add_f32 v80, v80, v29\n\t"
    "v_add_f32 v80, v80, v30\n\t"
    "v_add_f32 v80, v80, v31\n\t"
    "v_cvt_pk_bf16_f32 v48, v16, v17\n\t"
    "v_cvt_pk_bf16_f32 v49, v18, v19\n\t"
    "v_cvt_pk_bf16_f32 v50, v20, v21\n\t"
    "v_cvt_pk_bf16_f32 v51, v22, v23\n\t"
    "v_cvt_pk_bf16_f32 v52, v24, v25\n\t"
    "v_cvt_pk_bf16_f32 v53, v26, v27\n\t"
    "v_cvt_pk_bf16_f32 v54, v28, v29\n\t"
    "v_cvt_pk_bf16_f32 v55, v30, v31\n\t"
    "v_sub_f32 v32, v32, v2\n\t"
    "v_sub_f32 v33, v33, v2\n\t"
    "v_sub_f32 v34, v34, v2\n\t"
    "v_sub_f32 v35, v35, v2\n\t"
    "v_sub_f32 v36, v36, v2\n\t"
    "v_sub_f32 v37, v37, v2\n\t"
    "v_sub_f32 v38, v38, v2\n\t"
    "v_sub_f32 v39, v39, v2\n\t"
    "v_sub_f32 v40, v40, v2\n\t"
    "v_sub_f32 v41, v41, v2\n\t"
    "v_sub_f32 v42, v42, v2\n\t"
    "v_sub_f32 v43, v43, v2\n\t"
    "v_sub_f32 v44, v44, v2\n\t"
    "v_sub_f32 v45, v45, v2\n\t"
    "v_sub_f32 v46, v46, v2\n\t"
    "v_sub_f32 v47, v47, v2\n\t"
    "v_exp_f32 v32, v32\n\t"
    "v_exp_f32 v33, v33\n\t"
    "v_exp_f32 v34, v34\n\t"
    "v_exp_f32 v35, v35\n\t"
    "v_exp_f32 v36, v36\n\t"
    "v_exp_f32 v37, v37\n\t"
    "v_exp_f32 v38, v38\n\t"
    "v_exp_f32 v39, v39\n\t"
    "v_exp_f32 v40, v40\n\t"
    "v_exp_f32 v41, v41\n\t"
    "v_exp_f32 v42, v42\n\t"
    "v_exp_f32 v43, v43\n\t"
    "v_exp_f32 v44, v44\n\t"
    "v_exp_f32 v45, v45\n\t"
    "v_exp_f32 v46, v46\n\t"
    "v_exp_f32 v47, v47\n\t"
    "s_nop 0\n\t"
    "v_add_f32 v80, v80, v32\n\t"
    "v_add_f32 v80, v80, v33\n\t"
    "v_add_f32 v80, v80, v34\n\t"
    "v_add_f32 v80, v80, v35\n\t"
    "v_add_f32 v80, v80, v36\n\t"
    "v_add_f32 v80, v80, v37\n\t"
    "v_add_f32 v80, v80, v38\n\t"
    "v_add_f32 v80, v80, v39\n\t"
    "v_add_f32 v80, v80, v40\n\t"
    "v_add_f32 v80, v80, v41\n\t"
    "v_add_f32 v80, v80, v42\n\t"
    "v_add_f32 v80, v80, v43\n\t"
    "v_add_f32 v80, v80, v44\n\t"
    "v_add_f32 v80, v80, v45\n\t"
    "v_add_f32 v80, v80, v46\n\t"
    "v_add_f32 v80, v80, v47\n\t"
    "v_cvt_pk_bf16_f32 v56, v32, v33\n\t"
    "v_cvt_pk_bf16_f32 v57, v34, v35\n\t"
    "v_cvt_pk_bf16_f32 v58, v36, v37\n\t"
    "v_cvt_pk_bf16_f32 v59, v38, v39\n\t"
    "v_cvt_pk_bf16_f32 v60, v40, v41\n\t"
    "v_cvt_pk_bf16_f32 v61, v42, v43\n\t"
    "v_cvt_pk_bf16_f32 v62, v44, v45\n\t"
    "v_cvt_pk_bf16_f32 v63, v46, v47\n\t"
    "s_waitcnt lgkmcnt(0)\n\t"
    "s_nop 1\n\t"
    "v_mfma_f32_32x32x16_bf16 v[160:175], a[64:67], v[48:51], v[160:175]\n\t"
    "v_mfma_f32_32x32x16_bf16 v[176:191], a[68:71], v[48:51], v[176:191]\n\t"
    "v_mfma_f32_32x32x16_bf16 v[192:207], a[72:75], v[48:51], v[192:207]\n\t"
    "v_mfma_f32_32x32x16_bf16 v[208:223], a[76:79], v[48:51], v[208:223]\n\t"
    "v_mfma_f32_32x32x16_bf16 v[160:175], a[80:83], v[52:55], v[160:175]\n\t"
    "v_mfma_f32_32x32x16_bf16 v[176:191], a[84:87], v[52:55], v[176:191]\n\t"
    "v_mfma_f32_32x32x16_bf16 v[192:207], a[88:91], v[52:55], v[192:207]\n\t"
    "v_mfma_f32_32x32x16_bf16 v[208:223], a[92:95], v[52:55], v[208:223]\n\t"
    "v_mfma_f32_32x32x16_bf16 v[160:175], a[96:99], v[56:59], v[160:175]\n\t"
    "v_mfma_f32_32x32x16_bf16 v[176:191], a[100:103], v[56:59], v[176:191]\n\t"
    "v_mfma_f32_32x32x16_bf16 v[192:207], a[104:107], v[56:59], v[192:207]\n\t"
    "v_mfma_f32_32x32x16_bf16 v[208:223], a[108:111], v[56:59], v[208:223]\n\t"
    "v_mfma_f32_32x32x16_bf16 v[160:175], a[112:115], v[60:63], v[160:175]\n\t"
    "v_mfma_f32_32x32x16_bf16 v[176:191], a[116:119], v[60:63], v[176:191]\n\t"
    "v_mfma_f32_32x32x16_bf16 v[192:207], a[120:123], v[60:63], v[192:207]\n\t"
    "v_mfma_f32_32x32x16_bf16 v[208:223], a[124:127], v[60:63], v[208:223]\n\t"
    "s_nop 15\n\t"
    "s_barrier\n\t"
    "s_add_u32 s29, s29, 1\n\t"
    "s_cmp_lt_u32 s29, 16\n\t"
    "s_cbranch_scc1 LSLOOP_%=\n\t"
    "s_branch LNORM_%=\n\t"
    "LEND_%=:\n\t"
    "s_mov_b64 exec, -1\n\t"
    "s_waitcnt vmcnt(0) lgkmcnt(0)\n\t"
    :: "s"(x), "s"(kc), "s"(vc), "s"(wq), "s"(wo), "s"(out), "s"(ws), "s"(bid), "v"(tid)
    : "memory", "vcc", "scc", "v1", "v2", "v3", "v4", "v5", "v6", "v7", "v8", "v9", "v10", "v11", "v12", "v13", "v14", "v15", "v16", "v17", "v18", "v19", "v20", "v21", "v22", "v23", "v24", "v25", "v26", "v27", "v28", "v29", "v30", "v31", "v32", "v33", "v34", "v35", "v36", "v37", "v38", "v39", "v40", "v41", "v42", "v43", "v44", "v45", "v46", "v47", "v48", "v49", "v50", "v51", "v52", "v53", "v54", "v55", "v56", "v57", "v58", "v59", "v60", "v61", "v62", "v63", "v64", "v65", "v66", "v67", "v68", "v69", "v70", "v71", "v72", "v73", "v74", "v75", "v76", "v77", "v78", "v79", "v80", "v81", "v82", "v83", "v84", "v85", "v86", "v87", "v88", "v89", "v90", "v91", "v92", "v93", "v94", "v95", "v96", "v97", "v98", "v99", "v100", "v101", "v102", "v103", "v104", "v105", "v106", "v107", "v108", "v109", "v110", "v111", "v112", "v113", "v114", "v115", "v116", "v117", "v118", "v119", "v120", "v121", "v122", "v123", "v124", "v125", "v126", "v127", "v128", "v129", "v130", "v131", "v132", "v133", "v134", "v135", "v136", "v137", "v138", "v139", "v140", "v141", "v142", "v143", "v144", "v145", "v146", "v147", "v148", "v149", "v150", "v151", "v152", "v153", "v154", "v155", "v156", "v157", "v158", "v159", "v160", "v161", "v162", "v163", "v164", "v165", "v166", "v167", "v168", "v169", "v170", "v171", "v172", "v173", "v174", "v175", "v176", "v177", "v178", "v179", "v180", "v181", "v182", "v183", "v184", "v185", "v186", "v187", "v188", "v189", "v190", "v191", "v192", "v193", "v194", "v195", "v196", "v197", "v198", "v199", "v200", "v201", "v202", "v203", "v204", "v205", "v206", "v207", "v208", "v209", "v210", "v211", "v212", "v213", "v214", "v215", "v216", "v217", "v218", "v219", "v220", "v221", "v222", "v223", "v224", "v225", "v226", "v227", "v228", "v229", "v230", "v231", "v232", "v233", "v234", "v235", "v236", "v237", "v238", "v239", "v240", "v241", "v242", "v243", "v244", "v245", "v246", "v247", "v248", "v249", "v250", "v251", "v252", "v253", "v254", "v255", "a0", "a1", "a2", "a3", "a4", "a5", "a6", "a7", "a8", "a9", "a10", "a11", "a12", "a13", "a14", "a15", "a16", "a17", "a18", "a19", "a20", "a21", "a22", "a23", "a24", "a25", "a26", "a27", "a28", "a29", "a30", "a31", "a32", "a33", "a34", "a35", "a36", "a37", "a38", "a39", "a40", "a41", "a42", "a43", "a44", "a45", "a46", "a47", "a48", "a49", "a50", "a51", "a52", "a53", "a54", "a55", "a56", "a57", "a58", "a59", "a60", "a61", "a62", "a63", "a64", "a65", "a66", "a67", "a68", "a69", "a70", "a71", "a72", "a73", "a74", "a75", "a76", "a77", "a78", "a79", "a80", "a81", "a82", "a83", "a84", "a85", "a86", "a87", "a88", "a89", "a90", "a91", "a92", "a93", "a94", "a95", "a96", "a97", "a98", "a99", "a100", "a101", "a102", "a103", "a104", "a105", "a106", "a107", "a108", "a109", "a110", "a111", "a112", "a113", "a114", "a115", "a116", "a117", "a118", "a119", "a120", "a121", "a122", "a123", "a124", "a125", "a126", "a127", "a128", "a129", "a130", "a131", "a132", "a133", "a134", "a135", "a136", "a137", "a138", "a139", "a140", "a141", "a142", "a143", "a144", "a145", "a146", "a147", "a148", "a149", "a150", "a151", "a152", "a153", "a154", "a155", "a156", "a157", "a158", "a159", "a160", "a161", "a162", "a163", "a164", "a165", "a166", "a167", "a168", "a169", "a170", "a171", "a172", "a173", "a174", "a175", "a176", "a177", "a178", "a179", "a180", "a181", "a182", "a183", "a184", "a185", "a186", "a187", "a188", "a189", "a190", "a191", "a192", "a193", "a194", "a195", "a196", "a197", "a198", "a199", "a200", "a201", "a202", "a203", "a204", "a205", "a206", "a207", "a208", "a209", "a210", "a211", "a212", "a213", "a214", "a215", "a216", "a217", "a218", "a219", "a220", "a221", "a222", "a223", "a224", "a225", "a226", "a227", "a228", "a229", "a230", "a231", "a232", "a233", "a234", "a235", "a236", "a237", "a238", "a239", "a240", "a241", "a242", "a243", "a244", "a245", "a246", "a247", "a248", "a249", "a250", "a251", "a252", "a253", "a254", "a255", "s20", "s21", "s22", "s23", "s24", "s25", "s26", "s27", "s28", "s29", "s30", "s31", "s34", "s35", "s36", "s37", "s38", "s39", "s40", "s41", "s42", "s43", "s44", "s45", "s46", "s47", "s48", "s49", "s50", "s51", "s52", "s53", "s54", "s55", "s56", "s57", "s58", "s59", "s60", "s61", "s62", "s63", "s64", "s65", "s66", "s67", "s68", "s69", "s70", "s71", "s72", "s73", "s74", "s75", "s76", "s77", "s78", "s79", "s80", "s81", "s82", "s83", "s84", "s85", "s86", "s87", "s88", "s89", "s90", "s91", "s92", "s93", "s94", "s95", "s96", "s97", "s98", "s99");
}


extern "C" void kernel_launch(void* const* d_in, const int* in_sizes, int n_in, void* d_out, int out_size, void* d_ws, size_t ws_size, hipStream_t stream) {
  (void)in_sizes; (void)n_in; (void)out_size;
  const size_t WSOFF = (size_t)0 << 20;
  (void)hipFuncSetAttribute((const void*)gqa_main, hipFuncAttributeMaxDynamicSharedMemorySize, 163840);
  gqa_main<<<256, 256, 163840, stream>>>((const char*)d_in[0], (const char*)d_in[1], (const char*)d_in[2], (const char*)d_in[3], (const char*)d_in[4], (char*)d_out, (char*)d_ws + WSOFF);
}
```
